```python
import torch

def _softmax(x, axis=-1):
    return torch.softmax(x, dim=axis)
def _gelu(x):
    return torch.nn.functional.gelu(x, approximate="tanh")
def _silu(x):
    return torch.nn.functional.silu(x)
def _sigmoid(x):
    return torch.sigmoid(x)
def _layernorm(x, axis=-1, eps=1e-5):
    m = x.mean(dim=axis, keepdim=True)
    v = x.var(dim=axis, keepdim=True, unbiased=False)
    return (x - m) / torch.sqrt(v + eps)
def _scatter_add(tgt, idx, src, axis=0):
    return tgt.clone().index_add_(axis, idx, src)
def _segment_sum(src, seg, n):
    out = torch.zeros((n,) + src.shape[1:], dtype=src.dtype)
    return out.index_add_(0, seg, src)
def _pad(x, pw):
    flat = tuple(v for pair in reversed(pw) for v in pair)
    return torch.nn.functional.pad(x, flat)

kernel_name = 'concat_heads__gqa_block_cached__gqa_block_cached__near_ridge'

def setup_inputs():

    _w = (lambda a: (lambda t: t.to(torch.bfloat16) if t.is_floating_point() else t)(torch.as_tensor(a)))
    def _A_setup():
        B, Sq, D = 8, 64, 448
        Hq, Hkv, Sk, Dh = 16, 8, 64, 256
        assert Hq % Hkv == 0, (Hq, Hkv)
        return {
            "x": _w((lambda *s: torch.randn(*s, dtype=torch.bfloat16) * 0.8)(B, Sq, D)),
            "k_cache": _w((lambda *s: torch.randn(*s, dtype=torch.bfloat16) * 0.8)(B, Hkv, Sk, Dh)),
            "v_cache": _w((lambda *s: torch.randn(*s, dtype=torch.bfloat16) * 0.05)(B, Hkv, Sk, Dh)),
            "wq": _w((lambda *s: torch.randn(*s, dtype=torch.bfloat16) * 0.8)(D, Hq * Dh) * (D ** -0.5)),
            "wo": _w((lambda *s: torch.randn(*s, dtype=torch.bfloat16) * 0.05)(Hq * Dh, D) * ((Hq * Dh) ** -0.5)),
        }
    _A_in = _A_setup()

    def _B_setup():
        B, Sq, D = 8, 64, 448
        Hq, Hkv, Sk, Dh = 16, 8, 64, 256
        assert Hq % Hkv == 0, (Hq, Hkv)
        return {
            "x": _w((lambda *s: torch.randn(*s, dtype=torch.bfloat16) * 0.8)(B, Sq, D)),
            "k_cache": _w((lambda *s: torch.randn(*s, dtype=torch.bfloat16) * 0.8)(B, Hkv, Sk, Dh)),
            "v_cache": _w((lambda *s: torch.randn(*s, dtype=torch.bfloat16) * 0.05)(B, Hkv, Sk, Dh)),
            "wq": _w((lambda *s: torch.randn(*s, dtype=torch.bfloat16) * 0.8)(D, Hq * Dh) * (D ** -0.5)),
            "wo": _w((lambda *s: torch.randn(*s, dtype=torch.bfloat16) * 0.05)(Hq * Dh, D) * ((Hq * Dh) ** -0.5)),
        }
    _B_in = _B_setup()

    return {
        "A_x": _A_in["x"],
        "A_k_cache": _A_in["k_cache"],
        "A_v_cache": _A_in["v_cache"],
        "A_wq": _A_in["wq"],
        "A_wo": _A_in["wo"],
        "B_x": _B_in["x"],
        "B_k_cache": _B_in["k_cache"],
        "B_v_cache": _B_in["v_cache"],
        "B_wq": _B_in["wq"],
        "B_wo": _B_in["wo"],
    }

def reference(A_x, A_k_cache, A_v_cache, A_wq, A_wo, B_x, B_k_cache, B_v_cache, B_wq, B_wo):
    def _A_ref(x, k_cache, v_cache, wq, wo):
        B, Sq, D = x.shape
        Hq, Dh = 16, 256
        Hkv = k_cache.shape[1]
        rep = Hq // Hkv
        q = (lambda x, *d: x.permute(*d))((x @ wq).reshape(B, Sq, Hq, Dh), 0, 2, 1, 3)
        k = (lambda x, n, axis: torch.repeat_interleave(x, n, dim=axis))(k_cache, rep, axis=1)
        v = (lambda x, n, axis: torch.repeat_interleave(x, n, dim=axis))(v_cache, rep, axis=1)
        scores = torch.einsum("bhqd,bhkd->bhqk", q, k) * (Dh ** -0.5)
        p = _softmax(scores, axis=-1)
        o = torch.einsum("bhqk,bhkd->bhqd", p, v)
        return (lambda x, *d: x.permute(*d))(o, 0, 2, 1, 3).reshape(B, Sq, Hq * Dh) @ wo

    def _B_ref(x, k_cache, v_cache, wq, wo):
        B, Sq, D = x.shape
        Hq, Dh = 16, 256
        Hkv = k_cache.shape[1]
        rep = Hq // Hkv
        q = (lambda x, *d: x.permute(*d))((x @ wq).reshape(B, Sq, Hq, Dh), 0, 2, 1, 3)
        k = (lambda x, n, axis: torch.repeat_interleave(x, n, dim=axis))(k_cache, rep, axis=1)
        v = (lambda x, n, axis: torch.repeat_interleave(x, n, dim=axis))(v_cache, rep, axis=1)
        scores = torch.einsum("bhqd,bhkd->bhqk", q, k) * (Dh ** -0.5)
        p = _softmax(scores, axis=-1)
        o = torch.einsum("bhqk,bhkd->bhqd", p, v)
        return (lambda x, *d: x.permute(*d))(o, 0, 2, 1, 3).reshape(B, Sq, Hq * Dh) @ wo

    a = _A_ref(A_x, A_k_cache, A_v_cache, A_wq, A_wo)
    b = _B_ref(B_x, B_k_cache, B_v_cache, B_wq, B_wo)
    return torch.concatenate([a, b], axis=-1)
```

```cpp
#include <hip/hip_runtime.h>
#include <hip/hip_bf16.h>
typedef unsigned short u16;
__device__ inline float bf2f(u16 v){ return __uint_as_float(((unsigned)v)<<16); }
__device__ inline u16 f2bf(float f){ unsigned u=__float_as_uint(f); u += 0x7FFF + ((u>>16)&1); return (u16)(u>>16); }

__global__ void gemm_naive(const u16* A, const u16* B, u16* C, int M, int N, int K, int ldc){
  int n = blockIdx.x*blockDim.x+threadIdx.x; int m = blockIdx.y;
  if(n>=N) return;
  float acc=0;
  for(int k=0;k<K;k++) acc += bf2f(A[m*K+k])*bf2f(B[(size_t)k*N+n]);
  C[(size_t)m*ldc+n]=f2bf(acc);
}
__global__ void attn_naive(const u16* Q, const u16* Kc, const u16* Vc, u16* O){
  int q = blockIdx.x, h = blockIdx.y, b = blockIdx.z; int t = threadIdx.x;
  __shared__ float p[64];
  const u16* qr = Q + (size_t)(b*64+q)*4096 + h*256;
  const u16* kr = Kc + ((size_t)(b*8+h/2)*64 + t)*256;
  float s=0; for(int d=0;d<256;d++) s += bf2f(qr[d])*bf2f(kr[d]);
  s = bf2f(f2bf(s)); s = bf2f(f2bf(s*0.0625f));
  float m = s; for(int o=32;o;o>>=1) m = fmaxf(m, __shfl_xor(m,o));
  float e = __expf(s-m); float sum=e; for(int o=32;o;o>>=1) sum += __shfl_xor(sum,o);
  p[t] = bf2f(f2bf(e/sum));
  __syncthreads();
  const u16* vb = Vc + (size_t)(b*8+h/2)*64*256;
  for(int d=t; d<256; d+=64){ float a=0; for(int k=0;k<64;k++) a += p[k]*bf2f(vb[k*256+d]); O[(size_t)(b*64+q)*4096+h*256+d]=f2bf(a);}
}
extern "C" void kernel_launch(void* const* d_in, const int* in_sizes, int n_in,
                              void* d_out, int out_size, void* d_ws, size_t ws_size,
                              hipStream_t stream) {
  for(int br=0;br<2;br++){
    const u16* x=(const u16*)d_in[br*5+0]; const u16* k=(const u16*)d_in[br*5+1]; const u16* v=(const u16*)d_in[br*5+2];
    const u16* wq=(const u16*)d_in[br*5+3]; const u16* wo=(const u16*)d_in[br*5+4];
    u16* Q=(u16*)d_ws + (size_t)br*2*512*4096; u16* O=Q+512*4096;
    gemm_naive<<<dim3(16,512),256,0,stream>>>(x,wq,Q,512,4096,448,4096);
    attn_naive<<<dim3(64,16,8),64,0,stream>>>(Q,k,v,O);
    gemm_naive<<<dim3(7,512),64,0,stream>>>(O,wo,(u16*)d_out+br*448,512,448,4096,896);
  }
}
```

```cpp
#include <hip/hip_runtime.h>
#include <utility>

typedef unsigned int u32;
typedef unsigned long long u64;
typedef short s16x4 __attribute__((ext_vector_type(4)));
typedef short s16x8 __attribute__((ext_vector_type(8)));
typedef float f32x2 __attribute__((ext_vector_type(2)));
typedef float f32x4 __attribute__((ext_vector_type(4)));
typedef u32 u32x4 __attribute__((ext_vector_type(4)));
typedef __bf16 bf16x2 __attribute__((ext_vector_type(2)));
#define LAS __attribute__((address_space(3)))
#define GAS __attribute__((address_space(1)))
#define DEV __device__ __forceinline__
#define IC(n) std::integral_constant<int, n>{}

constexpr size_t WS_EPOCH = 0;
constexpr size_t WS_FLAG = 4096;
constexpr size_t WS_P = 65536;
constexpr size_t WS_X = WS_P + 256 * 8192;
constexpr int LDS_BYTES = 163840;
constexpr u32 SPIN_CAP = 1u << 20;
constexpr u32 CODE_LAST = 18432;
constexpr u32 PTAG = 0x80008000u;
constexpr int NPF = 2, KPF = 3;
constexpr int NCV0 = 2;

struct Params {
  const char* in[2][5];
  char* out;
  char* ws;
};

template <class F, int... Is> DEV void sfor_impl(F&& f, std::integer_sequence<int, Is...>) { (f(std::integral_constant<int, Is>{}), ...); }
template <int N, class F> DEV void sfor(F&& f) { sfor_impl(f, std::make_integer_sequence<int, N>{}); }

template <int N> DEV void wait_vm() { asm volatile("s_waitcnt vmcnt(%0)" ::"n"(N) : "memory"); }
DEV void bar() { asm volatile("s_barrier" ::: "memory"); }
DEV void bar_lds() { asm volatile("s_waitcnt lgkmcnt(0)\n\ts_barrier" ::: "memory"); }
DEV void sched0() { __builtin_amdgcn_sched_barrier(0); }
DEV void pin(s16x8& x) { asm volatile("" : "+v"(x)); }
DEV u32 opq(u32 x) { asm volatile("" : "+v"(x)); return x; }
template <int M, int N> DEV void sgb() { __builtin_amdgcn_sched_group_barrier(M, N, 0); }
constexpr int SG_VALU = 0x2, SG_MFMA = 0x8, SG_DSR = 0x100;

template <int SC1> DEV void dma_block(u32 v0, u32 v1, u32 lds, const char* base) {
  if constexpr (SC1 == 0)
    asm volatile("s_mov_b32 m0, %2\n\ts_nop 0\n\tglobal_load_lds_dwordx4 %0, %3\n\tglobal_load_lds_dwordx4 %1, %3 offset:1024"
                 ::"v"(v0), "v"(v1), "s"(lds), "s"(base) : "memory");
  else
    asm volatile("s_mov_b32 m0, %2\n\ts_nop 0\n\tglobal_load_lds_dwordx4 %0, %3 sc1\n\tglobal_load_lds_dwordx4 %1, %3 offset:1024 sc1"
                 ::"v"(v0), "v"(v1), "s"(lds), "s"(base) : "memory");
}
DEV void dma_word(u32 v, u32 lds, const char* base) {
  asm volatile("s_mov_b32 m0, %1\n\ts_nop 0\n\tglobal_load_lds_dword %0, %2" ::"v"(v), "s"(lds), "s"(base) : "memory");
}
DEV void ld_word_async(u32& d, u32 off, const char* base) { asm volatile("global_load_dword %0, %1, %2 sc1" : "=v"(d) : "v"(off), "s"(base) : "memory"); }
DEV void st_x4_sc1(u32x4 v, u32 off, char* base) { asm volatile("global_store_dwordx4 %0, %1, %2 sc1\n\ts_nop 1" ::"v"(off), "v"(v), "s"(base) : "memory"); }
DEV void st_word_sc1(u32 v, u32 off, char* base) { asm volatile("global_store_dword %0, %1, %2 sc1" ::"v"(off), "v"(v), "s"(base) : "memory"); }
template <int N> DEV bool wait_all_eq(u32 d, u32 want) {
  u64 m;
  asm volatile("s_waitcnt vmcnt(%3)\n\tv_cmp_ne_u32 %0, %1, %2" : "=s"(m) : "v"(d), "v"(want), "n"(N) : "memory");
  return m == 0;
}
template <int N> DEV u32 wait_tag(u32 d) { u32 r; asm volatile("s_waitcnt vmcnt(%2)\n\tv_xor_b32 %0, 0x80008000, %1" : "=v"(r) : "v"(d), "n"(N) : "memory"); return r; }
DEV void spin_all_eq(u32 off, const char* base, u32 want) {
  for (u32 spins = 0; spins < SPIN_CAP; ++spins) {
    u64 m; u32 d;
    asm volatile("global_load_dword %1, %2, %3 sc1\n\ts_waitcnt vmcnt(0)\n\tv_cmp_ne_u32 %0, %1, %4" : "=s"(m), "=&v"(d) : "v"(off), "s"(base), "v"(want) : "memory");
    if (m == 0) break;
  }
}

DEV s16x8 lds128(u32 a) { return *(const LAS s16x8*)(size_t)a; }
DEV s16x8 ldstr(u32 a) {
  s16x4 lo = __builtin_amdgcn_ds_read_tr16_b64_v4i16((LAS s16x4*)(size_t)a);
  s16x4 hi = __builtin_amdgcn_ds_read_tr16_b64_v4i16((LAS s16x4*)(size_t)(a + 512));
  return __builtin_shufflevector(lo, hi, 0, 1, 2, 3, 4, 5, 6, 7);
}
DEV u32 pk2(float a, float b) { f32x2 t = {a, b}; return __builtin_bit_cast(u32, __builtin_convertvector(t, bf16x2)); }
DEV f32x4 mfma16(s16x8 a, s16x8 b, f32x4 c) { return __builtin_amdgcn_mfma_f32_16x16x32_bf16(a, b, c, 0, 0, 0); }
DEV float vmax(float a, float b) { float r; asm("v_max_f32 %0, %1, %2" : "=v"(r) : "v"(a), "v"(b)); return r; }
DEV float vmax3(float a, float b, float c) { float r; asm("v_max3_f32 %0, %1, %2, %3" : "=v"(r) : "v"(a), "v"(b), "v"(c)); return r; }
DEV float xmax(float m) {
  u32 u = __float_as_uint(m);
  auto s = __builtin_amdgcn_permlane32_swap(u, u, false, false);
  u32 a = s[0], b = s[1];
  m = vmax(__uint_as_float(a), __uint_as_float(b));
  u = __float_as_uint(m);
  auto t = __builtin_amdgcn_permlane16_swap(u, u, false, false);
  a = t[0]; b = t[1];
  return vmax(__uint_as_float(a), __uint_as_float(b));
}
DEV float xsum(float m) {
  u32 u = __float_as_uint(m);
  auto s = __builtin_amdgcn_permlane32_swap(u, u, false, false);
  u32 a = s[0], b = s[1];
  m = __uint_as_float(a) + __uint_as_float(b);
  u = __float_as_uint(m);
  auto t = __builtin_amdgcn_permlane16_swap(u, u, false, false);
  a = t[0]; b = t[1];
  return __uint_as_float(a) + __uint_as_float(b);
}
DEV u32 umin(u32 a, u32 b) { return a < b ? a : b; }
DEV u32 umax(u32 a, u32 b) { return a > b ? a : b; }

constexpr int QG[7] = {0, 5, 10, 15, 0, 5, 10};
constexpr int KS0 = 15, QX0 = 10;
constexpr int US[8][4] = {{0, 1, 19, 2}, {3, 4, 5, 6}, {7, 8, 9, 14}, {15, 16, 17, 18}, {10, 11, 12, 13}, {0, 1, 19, 2}, {3, 4, 5, 6}, {7, 8, 9, 14}};
constexpr int UO0[4] = {2, 0, 1, 3};
constexpr int PS0 = 15, PS1 = 10;
constexpr int imin(int a, int b) { return a < b ? a : b; }
constexpr int imax(int a, int b) { return a > b ? a : b; }
constexpr int NEED[17] = {5, 10, 15, 20, 25, 30, 35, 35, 39, 43, 47, 51, 55, 59, 63, 67, 71};
constexpr int ALLOW[17] = {20, 20, 20, 25, 30, 35, 40, 45, 51, 51, 59, 59, 63, 67, 71, 75, 79};
constexpr int LOOK[17] = {5, 5, 5, 5, 5, 5, 5, 7, 7, 6, 6, 6, 6, 6, 6, 6, 6};
constexpr int ib(int k) { return k < 0 ? 0 : imax(ib(k - 1), imin(ALLOW[k], NEED[k] + LOOK[k])); }
constexpr int open_at(int k) { return 2 * (ib(k) - NEED[k]) + (k > 4 && NEED[k] <= ib(4) ? 1 : 0) + (k > 13 && NEED[k] <= ib(13) ? 1 : 0); }
static_assert(ib(4) < NEED[6] && ib(5) == 35 && ib(KPF - 1) == 5 * (KPF + 1), "");
constexpr int fin_part(int f) { return f < 12 ? f >> 2 : f < 24 ? (f - 12) >> 2 : 3; }
constexpr int fin_head(int f) { return f < 12 ? (f >> 1) & 1 : f < 24 ? 2 + ((f >> 1) & 1) : (f - 24) >> 1; }

__global__ __launch_bounds__(512, 2) void fused_gqa2(Params p) {
  extern __shared__ __attribute__((aligned(16))) char smem[];
  u64 pc0;
  asm volatile("s_getpc_b64 %0" : "=s"(pc0));
  u64 kx0, kq0;
  asm volatile("s_load_dwordx2 %0, %2, %3\n\ts_load_dwordx2 %1, %2, %3 offset:8" : "=&s"(kx0), "=&s"(kq0)
               : "s"((u64)__builtin_amdgcn_kernarg_segment_ptr()), "s"(((blockIdx.x >> 2) & 1) * 40));
  const u32 bid = blockIdx.x, xcd = bid & 7, idx = bid >> 3;
  const u32 br = xcd >> 2, hg = xcd & 3, b = idx & 7, hh = (idx >> 3) & 3, h = hg * 4 + hh;
  const u32 bp = b >> 1, cg = 2 * hh + (b & 1);
  const bool vact = cg < 7;
  const u32 lane = threadIdx.x & 63;
  const u32 wv = __builtin_amdgcn_readfirstlane(threadIdx.x >> 6);
  const u32 lds0 = (u32)(size_t)(LAS char*)smem;

  if (__builtin_expect(wv < 4, 1)) {
    const u32 L = wv;
    __builtin_amdgcn_s_setprio(2);
    const u32 r8 = lane >> 3, c = lane & 7, row = 16 * L + r8;
    const u32 cA0 = (c ^ (r8 >> 1)) * 16, cA1 = (c ^ (4 + (r8 >> 1))) * 16, cW0 = (c ^ (r8 & 2)) * 16, cW1 = (c ^ ((r8 & 2) | 4)) * 16;
    const u32 ox0 = row * 896 + cA0, ox1 = (row + 8) * 896 + cA1 - 1024;
    const u32 oq0 = row * 8192 + cW0, oq1 = (row + 8) * 8192 + cW1 - 1024;
    const u32 lb = lds0 + L * 2048;
    const u32 qrot = ((b + hh) & 3) * 7 / 4;
    const u32 xo0 = b * 64 * 896 + qrot * 128, qo0 = h * 512 + qrot * 64 * 8192;
    asm volatile("" ::"v"(ox0), "v"(ox1), "v"(oq0), "v"(oq1), "s"(lb), "s"(xo0), "s"(qo0));
    const char *pX, *pQ;
    asm volatile("s_waitcnt lgkmcnt(0)\n\ts_mov_b64 %0, %2\n\ts_mov_b64 %1, %3" : "=&s"(pX), "=&s"(pQ) : "s"(kx0), "s"(kq0));
    dma_block<0>(ox0, ox1, lb, pX + xo0);
    sfor<4>([&](auto T) { dma_block<0>(oq0, oq1, lb + (1 + T.value) * 8192, pQ + qo0 + T.value * 128); });
    sched0();
    const char *pK = p.in[br][2], *pV = p.in[br][3], *pO = p.in[br][4];
    char* ws = p.ws;
    const char* X = pX + (size_t)b * 64 * 896;
    const char* WQ = pQ + h * 512;
    const u32 ok0 = row * 512 + cA0, ok1 = (row + 8) * 512 + cA1 - 1024;
    const u32 oo0 = row * 896 + cW0, oo1 = (row + 8) * 896 + cW1 - 1024;
    const u32 op0 = L * 2048 + lane * 16;
    const char *KK, *VV, *WO, *PP;
    const u32 vrot = (bp + cg) & 3, krot = (hh & 1) * 2;

    u32 ep;
    auto issue = [&](auto I) {
      constexpr int i = I.value;
      if constexpr (i < 35) {
        constexpr int s = i / 5, t = i % 5;
        u32 ss = s + qrot; if (ss >= 7) ss -= 7;
        if constexpr (t == 0) dma_block<0>(ox0, ox1, lb + QG[s] * 8192, X + ss * 128);
        else dma_block<0>(oq0, oq1, lb + (QG[s] + t) * 8192, WQ + (size_t)ss * 64 * 8192 + (t - 1) * 128);
      } else if constexpr (i < 39) {
        const u32 kb = (i - 35) ^ krot;
        dma_block<0>(ok0, ok1, lb + (KS0 + kb) * 8192, KK + kb * 128);
      } else if constexpr (i < 71) {
        constexpr int u = (i - 39) / 4, t = u == 0 ? UO0[(i - 39) % 4] : (i - 39) % 4, j = u & 1;
        const u32 ss = ((u >> 1) + vrot) & 3;
        if constexpr (t < 2) dma_block<0>(ok0, ok1, lb + US[u][t] * 8192, VV + (size_t)(t * 8 + j) * 32768 + ss * 128);
        else dma_block<0>(oo0, oo1, lb + US[u][t] * 8192, WO + (size_t)((2 * j + t - 2) * 256 + ss * 64) * 896);
      } else {
        constexpr int t = i - 71;
        dma_block<1>(op0, op0, lb + ((t < 4 ? PS0 : PS1) + (t & 3)) * 8192, PP + (size_t)(((t >> 1) & 1) * 16 + (t >> 2) * 2 + (t & 1)) * 8192);
      }
    };
    auto issue_range = [&](auto A, auto B) { sfor<B.value - A.value>([&](auto J) { issue(IC(A.value + J.value)); }); };
    auto step = [&](auto K) {
      constexpr int k = K.value;
      issue_range(IC(ib(k - 1)), IC(ib(k)));
      wait_vm<open_at(k)>();
      bar();
    };

    issue_range(IC(5), IC(ib(0)));
    asm volatile("" : "+s"(pK), "+s"(pV), "+s"(pO), "+s"(ws));
    KK = pK + (size_t)(b * 8 + (h >> 1)) * 32768;
    VV = pV + (size_t)(bp * 16 + hg * 2) * 32768;
    WO = pO + (size_t)hg * 1024 * 896 + (vact ? cg : 0) * 128;
    PP = ws + WS_P + (size_t)((br * 8 + bp * 2) * 16 + hg * 4) * 8192;
    wait_vm<open_at(0)>();
    bar();
    sfor<4>([&](auto K) {
      constexpr int k = K.value + 1;
      if constexpr (k == KPF) {
        if (!vact) sfor<NPF>([&](auto J) { dma_word((((L >> 1) * 8 + (L & 1)) * 64 + J.value * 16) * 512 + lane * 128, lb + QG[k + 1] * 8192, VV); });
        issue_range(IC(ib(k - 1)), IC(ib(k)));
        if (!vact) wait_vm<open_at(k) + NPF>(); else wait_vm<open_at(k)>();
        bar();
      } else {
        step(IC(K.value + 1));
      }
    });
    ld_word_async(ep, 0u, ws + WS_EPOCH + (br * 4 + bp) * 256);
    step(IC(5));
    if (vact) {
      issue_range(IC(ib(5)), IC(ib(6)));
      const u32 tag = wait_tag<open_at(6)>(ep);
      bar();
      sfor<7>([&](auto K) { step(IC(K.value + 7)); });
      const char* FL = ws + WS_FLAG;
      const u32 fo = ((br * 4 + bp) * 4 + ((lane >> 2) & 3)) * 128 + (lane & 3) * 4;
      u32 fl;
      ld_word_async(fl, fo, FL);
      step(IC(14)); step(IC(15)); step(IC(16));
      issue_range(IC(ib(16)), IC(79));
      __builtin_amdgcn_s_setprio(3);
      const u32 sgn = (u32)((int)tag >> 31);
      const bool neg = __builtin_amdgcn_readfirstlane(sgn) != 0;
      auto check = [&](auto H) {
        constexpr int hf = H.value;
        const u32 pv = opq(lds0 + op0 + (hf ? PS1 : PS0) * 8192);
        for (u32 spins = 0;; ++spins) {
          u32x4 v[8];
          sfor<4>([&](auto T) {
            if constexpr (hf) wait_vm<6 - 2 * T.value>(); else if constexpr (T.value == 0) wait_vm<8>();
            v[2 * T.value] = __builtin_bit_cast(u32x4, lds128(pv + T.value * 8192));
            v[2 * T.value + 1] = __builtin_bit_cast(u32x4, lds128(pv + T.value * 8192 + 1024));
          });
          u32 acc[2] = {sgn, sgn};
          if (neg) sfor<16>([&](auto I) { constexpr int i = I.value; acc[i & 1] = __builtin_amdgcn_bitop3_b32(acc[i & 1], v[i >> 1][2 * (i & 1)], v[i >> 1][2 * (i & 1) + 1], 0x80); });
          else sfor<16>([&](auto I) { constexpr int i = I.value; acc[i & 1] = __builtin_amdgcn_bitop3_b32(acc[i & 1], v[i >> 1][2 * (i & 1)], v[i >> 1][2 * (i & 1) + 1], 0xFE); });
          if (__all((((acc[0] ^ sgn) | (acc[1] ^ sgn)) & PTAG) == 0) || spins >= SPIN_CAP) break;
          issue_range(IC(71 + 4 * hf), IC(75 + 4 * hf));
          wait_vm<0>();
        }
      };
      check(IC(0));
      bar();
      check(IC(1));
      if (!wait_all_eq<0>(fl, tag)) spin_all_eq(fo, FL, tag);
      bar();
    } else {
      issue_range(IC(ib(5)), IC(39));
      const u32 tag = wait_tag<8>(ep);
      bar();
      bar();
      wait_vm<0>();
      bar();
      char* ibx = ws + WS_X + (size_t)((br * 4 + bp) * 32 + hg) * 16384;
      const u32x4 e = {~0u, ~0u, ~0u, ~0u};
      const u32 mo = L * 4096 + lane * 16;
      sfor<3>([&](auto D) { st_x4_sc1(e, mo + D.value * 1024, ibx); });
      bar();
      for (u32 c2 = 1; c2 < 7; ++c2) sfor<3>([&](auto D) { st_x4_sc1(e, mo + D.value * 1024, ibx + (size_t)c2 * 65536); });
      wait_vm<0>();
      st_word_sc1(tag, L * 4, ws + WS_FLAG + ((br * 4 + bp) * 4 + hg) * 128);
    }
    return;
  }

  asm volatile("s_waitcnt lgkmcnt(0)" ::"s"(kx0), "s"(kq0));
  const u32 w = wv - 4;
  u32 cpf = 0;
  if (w == 0) cpf = *(const GAS u32*)((pc0 & ~127ull) + umin((idx % 5) * 4096 + lane * 64, CODE_LAST));
  char* xbase = p.ws + WS_X + (size_t)((br * 4 + bp) * 8 + cg) * 65536;
  char* pbase = p.ws + WS_P + (size_t)((br * 8 + b) * 16 + h) * 8192;
  char* obase = p.out;
  char* ebase = p.ws + WS_EPOCH + (br * 4 + bp) * 256;
  asm volatile("" : "+s"(xbase), "+s"(pbase), "+s"(obase), "+s"(ebase));
  const u32 epoch = __builtin_amdgcn_raw_buffer_load_b32(__builtin_amdgcn_make_buffer_rsrc(ebase, 0, 4, 0x00020000), (int)opq(0), 0, 16);
  __amdgpu_buffer_rsrc_t rx = __builtin_amdgcn_make_buffer_rsrc(xbase, 0, 65536, 0x00020000);
  const int xo = (int)opq(w * 4096 + lane * 16 + hg * 16384);

  const u32 i16 = lane & 15, g = lane >> 4, q4 = i16 >> 2, pp = lane & 3;
  const u32 a16 = lds0 + i16 * 128 + ((g ^ (i16 >> 1)) * 16);
  const u32 aT = lds0 + (8 * g + q4) * 128 + (((pp >> 1) ^ ((q4 & 2) | ((g & 1) << 2))) * 16) + 8 * (pp & 1);
  const u32 bb = w >> 1, ct = w & 1;
  u32 wa[2][3], va[2][2], kr[4];
  sfor<2>([&](auto C) {
    sfor<3>([&](auto R) { wa[C.value][R.value] = opq((aT ^ (64 * ct + 32 * C.value)) + R.value * 65536); });
    sfor<2>([&](auto R) { va[R.value][C.value] = opq((a16 ^ (64 * C.value)) + bb * 8192 + R.value * 81920); });
  });
  const u32 pown = opq(lds0 + lane * 16 + (PS1 + 2 * bb) * 8192 + hg * 2048);
  const u32 aK = lds0 + KS0 * 8192 + i16 * 128 + (((g >> 1) ^ (i16 >> 1)) * 16) + (g & 1) * 8;
  sfor<4>([&](auto I) { kr[I.value] = opq(aK ^ (32 * I.value)); });
  const u32 qwr = opq(lds0 + QX0 * 8192 + w * 2048 + lane * 16), qrd = opq(lds0 + (QX0 + w) * 8192 + lane * 16);
  __amdgpu_buffer_rsrc_t rp = __builtin_amdgcn_make_buffer_rsrc(pbase, 0, 8192, 0x00020000);
  const int po = (int)opq(w * 2048 + lane * 16);
  __amdgpu_buffer_rsrc_t ro = __builtin_amdgcn_make_buffer_rsrc(obase, 0, 512 * 1792, 0x00020000);
  const int oo = (int)opq(((2 * bp + bb) * 64 + 16 * hg + i16) * 1792 + (br * 448 + 64 * cg + 32 * ct + 16 * (g & 1) + 8 * (g >> 1)) * 2);

  auto pk44 = [](const f32x4& a, const f32x4& c) { return u32x4{pk2(a[0], a[1]), pk2(a[2], a[3]), pk2(c[0], c[1]), pk2(c[2], c[3])}; };
  auto weave = []() { sfor<12>([&](auto) { sgb<SG_DSR, 1>(); sgb<SG_MFMA, 1>(); }); sgb<SG_MFMA, 4>(); };
  s16x8 A[2][4], B[2][4];
  f32x4 sa[4] = {};
  {
    f32x4 qa[4][4] = {};
    u32 tq[2][4], xr[2][2];
    sfor<2>([&](auto H) {
      sfor<4>([&](auto C) { tq[H.value][C.value] = opq((aT ^ (32 * C.value)) + (w + 1) * 8192 + H.value * 81920); });
      sfor<2>([&](auto K) { xr[H.value][K.value] = opq((a16 ^ (64 * K.value)) + H.value * 81920); });
    });
    auto ld = [&](auto G) {
      constexpr int gs = G.value, s = gs >> 1, ks = gs & 1, hi = QG[s] >= 10, o = (QG[s] - 10 * hi) * 8192;
      sfor<4>([&](auto I) {
        A[ks][I.value] = ldstr(tq[hi][I.value] + o + ks * 4096);
        B[ks][I.value] = lds128(xr[hi][ks] + o + I.value * 2048);
      });
    };
    bar();
    __builtin_amdgcn_s_setprio(3);
    ld(IC(0));
    sfor<14>([&](auto G) {
      constexpr int gs = G.value, bf = gs & 1;
      sched0();
      if constexpr (bf == 1 && gs < 13) { bar(); sched0(); }
      if constexpr (gs < 13) ld(IC(gs + 1));
      sfor<16>([&](auto I) { constexpr int d = I.value >> 2, q = I.value & 3; qa[d][q] = mfma16(A[bf][d], B[bf][q], qa[d][q]); });
      if constexpr (gs < 13) weave();
    });
    sched0(); bar_lds(); sched0();
    sfor<8>([&](auto I) {
      constexpr int m = I.value >> 2, q = I.value & 3;
      *(LAS u32x4*)(size_t)(qwr + q * 8192 + m * 1024) = pk44(qa[2 * m][q], qa[2 * m + 1][q]);
    });
    sched0(); bar_lds(); sched0();
    s16x8 KF[8], QF[8];
    auto ldk = [&](auto F) {
      constexpr int f = F.value, u = f >> 2, o = (u >> 1) * 8192 + (f & 3) * 2048;
      if constexpr ((f & 3) == 0) QF[u] = lds128(qrd + u * 1024);
      s16x4 lo = *(const LAS s16x4*)(size_t)(kr[2 * (u & 1)] + o), hi = *(const LAS s16x4*)(size_t)(kr[2 * (u & 1) + 1] + o);
      KF[f & 7] = __builtin_shufflevector(lo, hi, 0, 1, 2, 3, 4, 5, 6, 7);
    };
    sfor<6>([&](auto F) { ldk(F); sched0(); });
    sfor<32>([&](auto F) {
      constexpr int f = F.value;
      sched0();
      if constexpr (f + 6 < 32) ldk(IC(f + 6));
      sa[f & 3] = mfma16(KF[f & 7], QF[f >> 2], sa[f & 3]);
    });
  }
  asm volatile("s_nop 7\n\ts_nop 3" : "+v"(sa[0]), "+v"(sa[1]), "+v"(sa[2]), "+v"(sa[3]));

  u32 ptg;
  {
    const float cs = 0.0625f * 1.44269504088896340736f;
    float m = vmax3(vmax3(sa[0][0], sa[0][1], sa[0][2]), vmax3(sa[0][3], sa[1][0], sa[1][1]), vmax3(sa[1][2], sa[1][3], sa[2][0]));
    m = vmax3(m, vmax3(sa[2][1], sa[2][2], sa[2][3]), vmax3(sa[3][0], sa[3][1], vmax(sa[3][2], sa[3][3])));
    m = xmax(m) * cs;
    ptg = (u32)((int)~epoch >> 31) & PTAG;
    asm volatile("" ::"v"(cpf));
    float e[16];
    sfor<16>([&](auto I) { constexpr int i = I.value; e[i] = __builtin_amdgcn_exp2f(fmaf(sa[i >> 2][i & 3], cs, -m)); });
    const float sum = (((e[0] + e[1]) + (e[2] + e[3])) + ((e[4] + e[5]) + (e[6] + e[7]))) + (((e[8] + e[9]) + (e[10] + e[11])) + ((e[12] + e[13]) + (e[14] + e[15])));
    const float r = __builtin_amdgcn_rcpf(xsum(sum));
    sfor<2>([&](auto U) {
      u32x4 pf;
      sfor<4>([&](auto I) { pf[I.value] = (pk2(e[8 * U.value + 2 * I.value] * r, e[8 * U.value + 2 * I.value + 1] * r) & ~PTAG) | ptg; });
      __builtin_amdgcn_raw_buffer_store_b128(pf, rp, po + U.value * 1024, 0, 16);
    });
  }
  sched0(); bar(); sched0();
  if (!vact) return;

  f32x4 xa[4][4][2] = {};
  s16x8 PF[4], XF[16];
  auto ldv = [&](auto G) {
    constexpr int gv = G.value, u = gv >> 1, ks = gv & 1, hi = US[u][0] >= 10, vo = (US[u][0] - 10 * hi) * 8192;
    sfor<4>([&](auto I) {
      constexpr int sl = US[u][2 + (I.value >> 1)];
      A[ks][I.value] = lds128(va[hi][ks] + vo + I.value * 2048);
      B[ks][I.value] = ldstr(wa[I.value & 1][sl >> 3] + (sl & 7) * 8192 + ks * 4096);
    });
  };
  auto cvx = [&](auto I) {
    constexpr int i = I.value;
    u32x4 t = pk44(xa[i >> 2][i & 2][i & 1], xa[i >> 2][(i & 2) + 1][i & 1]);
    sfor<4>([&](auto E) { t[E.value] ^= ptg; });
    XF[i] = __builtin_bit_cast(s16x8, t);
  };
  const u32 pa[4] = {opq(pown ^ 2048), opq(pown ^ 4096), opq(pown ^ 6144), pown};
  auto ldp = [&](auto F) {
    constexpr int f = F.value, hd = fin_head(f);
    PF[f & 3] = lds128(pa[fin_part(f)] + ((hd & 1) + (hd < 2 ? PS0 - PS1 : 0)) * 8192 + (f & 1) * 1024);
  };
  ldv(IC(0));
  sfor<16>([&](auto G) {
    constexpr int gv = G.value, bf = gv & 1, j = (gv >> 1) & 1;
    sched0();
    if constexpr (bf == 1 && gv < 15) { bar(); sched0(); }
    if constexpr (gv < 15) ldv(IC(gv + 1));
    if constexpr (gv >= 14) sfor<4>([&](auto I) { cvx(IC(4 * (gv - 14) + I.value)); });
    sfor<16>([&](auto I) {
      constexpr int e2 = I.value >> 3, kt = (I.value >> 1) & 3, c2 = I.value & 1;
      xa[2 * j + e2][kt][c2] = mfma16(A[bf][kt], B[bf][2 * e2 + c2], xa[2 * j + e2][kt][c2]);
    });
    if constexpr (gv == 14) { sfor<12>([&](auto) { sgb<SG_DSR, 1>(); sgb<SG_MFMA, 1>(); sgb<SG_VALU, 2>(); }); sfor<4>([&](auto) { sgb<SG_MFMA, 1>(); sgb<SG_VALU, 2>(); }); }
    else if constexpr (gv == 15) { sfor<16>([&](auto) { sgb<SG_MFMA, 1>(); sgb<SG_VALU, 2>(); }); }
    else weave();
  });
  {
    sched0(); bar();
    sfor<4>([&](auto F) { ldp(F); });
    sched0();
    sfor<NCV0>([&](auto I) { cvx(IC(8 + I.value)); pin(XF[8 + I.value]); });
    f32x4 oa[4][2] = {};
    auto send = [&](auto D) {
      constexpr int d = D.value;
      u32x4 a = pk44(oa[d - 1][0], oa[d - 1][1]);
      sfor<4>([&](auto E) { a[E.value] = umin(a[E.value], 0xFFFFFFFEu); });
      __builtin_amdgcn_raw_buffer_store_b128(a, rx, (xo ^ (d * 16384)) + (d - 1) * 1024, 0, 16);
    };
    sfor<32>([&](auto F) {
      constexpr int f = F.value, part = fin_part(f), x = 4 * fin_head(f) + 2 * (f & 1);
      sched0();
      if constexpr (f == 8) { bar(); sched0(); }
      const s16x8 pf = PF[f & 3];
      if constexpr (f + 4 < 32) ldp(IC(f + 4));
      oa[part][0] = mfma16(XF[x], pf, oa[part][0]);
      if constexpr (f + NCV0 < 8) { cvx(IC(8 + NCV0 + f)); sgb<SG_DSR, 1>(); sgb<SG_MFMA, 1>(); sgb<SG_VALU, 4>(); sgb<SG_MFMA, 1>(); sgb<SG_VALU, 4>(); }
      oa[part][1] = mfma16(XF[x + 1], pf, oa[part][1]);
      if constexpr (f == 17 || f == 21 || f == 25) send(IC((f - 13) / 4));
    });
    asm volatile("" : "+v"(oa[3][0]), "+v"(oa[3][1]));
    __builtin_amdgcn_s_sleep(4);
    u32x4 v[3];
    auto fetch = [&]() { sfor<3>([&](auto D) { v[D.value] = __builtin_amdgcn_raw_buffer_load_b128(rx, xo + D.value * 1024, 0, 16); }); };
    fetch();
#pragma nounroll
    for (u32 spins = 0; spins < SPIN_CAP; ++spins) {
      u32 mx = 0;
      sfor<3>([&](auto D) { sfor<4>([&](auto E) { mx = umax(mx, v[D.value][E.value]); }); });
      if (__all(mx != ~0u)) break;
      asm volatile("" ::: "memory");
      fetch();
    }
    float o[8];
    sfor<8>([&](auto I) {
      constexpr int i = I.value;
      auto up = [&](u32 x) { return __uint_as_float((i & 1) ? (x & 0xFFFF0000u) : (x << 16)); };
      o[i] = ((oa[3][i >> 2][i & 3] + up(v[0][i >> 1])) + up(v[1][i >> 1])) + up(v[2][i >> 1]);
    });
    const auto s0 = __builtin_amdgcn_permlane16_swap(pk2(o[0], o[1]), pk2(o[4], o[5]), false, false);
    const auto s1 = __builtin_amdgcn_permlane16_swap(pk2(o[2], o[3]), pk2(o[6], o[7]), false, false);
    const u32 w0 = s0[0], w1 = s1[0], w2 = s0[1], w3 = s1[1];
    __builtin_amdgcn_raw_buffer_store_b128(u32x4{w0, w1, w2, w3}, ro, oo, 0, 0);
    if (hg == 0 && cg == 0 && threadIdx.x == 256)
      __hip_atomic_fetch_xor((GAS u32*)ebase, PTAG, __ATOMIC_RELAXED, __HIP_MEMORY_SCOPE_AGENT);
  }
}

extern "C" void kernel_launch(void* const* d_in, const int* in_sizes, int n_in, void* d_out, int out_size, void* d_ws,
                              size_t ws_size, hipStream_t stream) {
  Params p;
  for (int br = 0; br < 2; br++) {
    p.in[br][0] = (const char*)d_in[br * 5 + 0];
    p.in[br][1] = (const char*)d_in[br * 5 + 3];
    p.in[br][2] = (const char*)d_in[br * 5 + 1];
    p.in[br][3] = (const char*)d_in[br * 5 + 2];
    p.in[br][4] = (const char*)d_in[br * 5 + 4];
  }
  p.out = (char*)d_out;
  p.ws = (char*)d_ws;
  (void)hipFuncSetAttribute((const void*)fused_gqa2, hipFuncAttributeMaxDynamicSharedMemorySize, LDS_BYTES);
  fused_gqa2<<<256, 512, LDS_BYTES, stream>>>(p);
}
```

```cpp
#include <hip/hip_runtime.h>
#include <utility>

typedef unsigned int u32;
typedef unsigned long long u64;
typedef short s16x4 __attribute__((ext_vector_type(4)));
typedef short s16x8 __attribute__((ext_vector_type(8)));
typedef float f32x2 __attribute__((ext_vector_type(2)));
typedef float f32x4 __attribute__((ext_vector_type(4)));
typedef u32 u32x4 __attribute__((ext_vector_type(4)));
typedef __bf16 bf16x2 __attribute__((ext_vector_type(2)));
#define LAS __attribute__((address_space(3)))
#define GAS __attribute__((address_space(1)))
#define DEV __device__ __forceinline__
#define IC(n) std::integral_constant<int, n>{}

constexpr size_t WS_EPOCH = 0;
constexpr size_t WS_FLAG = 4096;
constexpr size_t WS_P = 65536;
constexpr size_t WS_X = WS_P + 256 * 8192;
constexpr int LDS_BYTES = 163840;
constexpr u32 SPIN_CAP = 1u << 20;
constexpr u32 CODE_LAST = 18432;
constexpr u32 PTAG = 0x80008000u;
constexpr int NPF = 2, KPF = 3;
constexpr int NCV0 = 4;

struct Params {
  const char* in[2][5];
  char* out;
  char* ws;
};

template <class F, int... Is> DEV void sfor_impl(F&& f, std::integer_sequence<int, Is...>) { (f(std::integral_constant<int, Is>{}), ...); }
template <int N, class F> DEV void sfor(F&& f) { sfor_impl(f, std::make_integer_sequence<int, N>{}); }

template <int N> DEV void wait_vm() { asm volatile("s_waitcnt vmcnt(%0)" ::"n"(N) : "memory"); }
DEV void bar() { asm volatile("s_barrier" ::: "memory"); }
DEV void bar_lds() { asm volatile("s_waitcnt lgkmcnt(0)\n\ts_barrier" ::: "memory"); }
DEV void sched0() { __builtin_amdgcn_sched_barrier(0); }
DEV void pin(s16x8& x) { asm volatile("" : "+v"(x)); }
DEV u32 opq(u32 x) { asm volatile("" : "+v"(x)); return x; }
template <int M, int N> DEV void sgb() { __builtin_amdgcn_sched_group_barrier(M, N, 0); }
constexpr int SG_VALU = 0x2, SG_MFMA = 0x8, SG_DSR = 0x100;

template <int SC1> DEV void dma_block(u32 v0, u32 v1, u32 lds, const char* base) {
  if constexpr (SC1 == 0)
    asm volatile("s_mov_b32 m0, %2\n\ts_nop 0\n\tglobal_load_lds_dwordx4 %0, %3\n\tglobal_load_lds_dwordx4 %1, %3 offset:1024"
                 ::"v"(v0), "v"(v1), "s"(lds), "s"(base) : "memory");
  else
    asm volatile("s_mov_b32 m0, %2\n\ts_nop 0\n\tglobal_load_lds_dwordx4 %0, %3 sc1\n\tglobal_load_lds_dwordx4 %1, %3 offset:1024 sc1"
                 ::"v"(v0), "v"(v1), "s"(lds), "s"(base) : "memory");
}
DEV void dma_word(u32 v, u32 lds, const char* base) {
  asm volatile("s_mov_b32 m0, %1\n\ts_nop 0\n\tglobal_load_lds_dword %0, %2" ::"v"(v), "s"(lds), "s"(base) : "memory");
}
DEV void ld_word_async(u32& d, u32 off, const char* base) { asm volatile("global_load_dword %0, %1, %2 sc1" : "=v"(d) : "v"(off), "s"(base) : "memory"); }
DEV void st_x4_sc1(u32x4 v, u32 off, char* base) { asm volatile("global_store_dwordx4 %0, %1, %2 sc1\n\ts_nop 1" ::"v"(off), "v"(v), "s"(base) : "memory"); }
DEV void st_word_sc1(u32 v, u32 off, char* base) { asm volatile("global_store_dword %0, %1, %2 sc1" ::"v"(off), "v"(v), "s"(base) : "memory"); }
template <int N> DEV bool wait_all_eq(u32 d, u32 want) {
  u64 m;
  asm volatile("s_waitcnt vmcnt(%3)\n\tv_cmp_ne_u32 %0, %1, %2" : "=s"(m) : "v"(d), "v"(want), "n"(N) : "memory");
  return m == 0;
}
template <int N> DEV u32 wait_tag(u32 d) { u32 r; asm volatile("s_waitcnt vmcnt(%2)\n\tv_xor_b32 %0, 0x80008000, %1" : "=v"(r) : "v"(d), "n"(N) : "memory"); return r; }
DEV void spin_all_eq(u32 off, const char* base, u32 want) {
  for (u32 spins = 0; spins < SPIN_CAP; ++spins) {
    u64 m; u32 d;
    asm volatile("global_load_dword %1, %2, %3 sc1\n\ts_waitcnt vmcnt(0)\n\tv_cmp_ne_u32 %0, %1, %4" : "=s"(m), "=&v"(d) : "v"(off), "s"(base), "v"(want) : "memory");
    if (m == 0) break;
  }
}

DEV s16x8 lds128(u32 a) { return *(const LAS s16x8*)(size_t)a; }
DEV s16x8 ldstr(u32 a) {
  s16x4 lo = __builtin_amdgcn_ds_read_tr16_b64_v4i16((LAS s16x4*)(size_t)a);
  s16x4 hi = __builtin_amdgcn_ds_read_tr16_b64_v4i16((LAS s16x4*)(size_t)(a + 512));
  return __builtin_shufflevector(lo, hi, 0, 1, 2, 3, 4, 5, 6, 7);
}
DEV u32 pk2(float a, float b) { f32x2 t = {a, b}; return __builtin_bit_cast(u32, __builtin_convertvector(t, bf16x2)); }
DEV f32x4 mfma16(s16x8 a, s16x8 b, f32x4 c) { return __builtin_amdgcn_mfma_f32_16x16x32_bf16(a, b, c, 0, 0, 0); }
DEV float vmax(float a, float b) { float r; asm("v_max_f32 %0, %1, %2" : "=v"(r) : "v"(a), "v"(b)); return r; }
DEV float vmax3(float a, float b, float c) { float r; asm("v_max3_f32 %0, %1, %2, %3" : "=v"(r) : "v"(a), "v"(b), "v"(c)); return r; }
DEV float xmax(float m) {
  u32 u = __float_as_uint(m);
  auto s = __builtin_amdgcn_permlane32_swap(u, u, false, false);
  u32 a = s[0], b = s[1];
  m = vmax(__uint_as_float(a), __uint_as_float(b));
  u = __float_as_uint(m);
  auto t = __builtin_amdgcn_permlane16_swap(u, u, false, false);
  a = t[0]; b = t[1];
  return vmax(__uint_as_float(a), __uint_as_float(b));
}
DEV float xsum(float m) {
  u32 u = __float_as_uint(m);
  auto s = __builtin_amdgcn_permlane32_swap(u, u, false, false);
  u32 a = s[0], b = s[1];
  m = __uint_as_float(a) + __uint_as_float(b);
  u = __float_as_uint(m);
  auto t = __builtin_amdgcn_permlane16_swap(u, u, false, false);
  a = t[0]; b = t[1];
  return __uint_as_float(a) + __uint_as_float(b);
}
DEV u32 umin(u32 a, u32 b) { return a < b ? a : b; }
DEV u32 umax(u32 a, u32 b) { return a > b ? a : b; }

constexpr int QG[7] = {0, 5, 10, 15, 0, 5, 10};
constexpr int KS0 = 15, QX0 = 10;
constexpr int US[8][4] = {{0, 1, 19, 2}, {3, 4, 5, 6}, {7, 8, 9, 14}, {15, 16, 17, 18}, {10, 11, 12, 13}, {0, 1, 19, 2}, {3, 4, 5, 6}, {7, 8, 9, 14}};
constexpr int UO0[4] = {2, 0, 1, 3};
constexpr int PS0 = 15, PS1 = 10;
constexpr int imin(int a, int b) { return a < b ? a : b; }
constexpr int imax(int a, int b) { return a > b ? a : b; }
constexpr int NEED[17] = {5, 10, 15, 20, 25, 30, 35, 35, 39, 43, 47, 51, 55, 59, 63, 67, 71};
constexpr int ALLOW[17] = {20, 20, 20, 25, 30, 35, 40, 45, 51, 51, 59, 59, 63, 67, 71, 75, 79};
constexpr int LOOK[17] = {5, 5, 5, 5, 5, 5, 5, 7, 7, 6, 6, 6, 6, 6, 6, 6, 6};
constexpr int ib(int k) { return k < 0 ? 0 : imax(ib(k - 1), imin(ALLOW[k], NEED[k] + LOOK[k])); }
constexpr int open_at(int k) { return 2 * (ib(k) - NEED[k]) + (k > 4 && NEED[k] <= ib(4) ? 1 : 0); }
static_assert(ib(4) < NEED[6] && ib(5) == 35 && ib(KPF - 1) == 5 * (KPF + 1), "");
constexpr int fin_part(int f) { return f < 12 ? f >> 2 : f < 24 ? (f - 12) >> 2 : 3; }
constexpr int fin_head(int f) { return f < 12 ? (f >> 1) & 1 : f < 24 ? 2 + ((f >> 1) & 1) : (f - 24) >> 1; }

__global__ __launch_bounds__(512, 2) void fused_gqa2(Params p) {
  extern __shared__ __attribute__((aligned(16))) char smem[];
  u64 pc0;
  asm volatile("s_getpc_b64 %0" : "=s"(pc0));
  u64 kx0, kq0;
  asm volatile("s_load_dwordx2 %0, %2, %3\n\ts_load_dwordx2 %1, %2, %3 offset:8" : "=&s"(kx0), "=&s"(kq0)
               : "s"((u64)__builtin_amdgcn_kernarg_segment_ptr()), "s"(((blockIdx.x >> 2) & 1) * 40));
  const u32 bid = blockIdx.x, xcd = bid & 7, idx = bid >> 3;
  const u32 br = xcd >> 2, hg = xcd & 3, b = idx & 7, hh = (idx >> 3) & 3, h = hg * 4 + hh;
  const u32 bp = b >> 1, cg = 2 * hh + (b & 1);
  const bool vact = cg < 7;
  const u32 lane = threadIdx.x & 63;
  const u32 wv = __builtin_amdgcn_readfirstlane(threadIdx.x >> 6);
  const u32 lds0 = (u32)(size_t)(LAS char*)smem;

  if (__builtin_expect(wv < 4, 1)) {
    const u32 L = wv;
    __builtin_amdgcn_s_setprio(2);
    const u32 r8 = lane >> 3, c = lane & 7, row = 16 * L + r8;
    const u32 cA0 = (c ^ (r8 >> 1)) * 16, cA1 = (c ^ (4 + (r8 >> 1))) * 16, cW0 = (c ^ (r8 & 2)) * 16, cW1 = (c ^ ((r8 & 2) | 4)) * 16;
    const u32 ox0 = row * 896 + cA0, ox1 = (row + 8) * 896 + cA1 - 1024;
    const u32 oq0 = row * 8192 + cW0, oq1 = (row + 8) * 8192 + cW1 - 1024;
    const u32 lb = lds0 + L * 2048;
    const u32 qrot = ((b + hh) & 3) * 7 / 4;
    const u32 xo0 = b * 64 * 896 + qrot * 128, qo0 = h * 512 + qrot * 64 * 8192;
    asm volatile("" ::"v"(ox0), "v"(ox1), "v"(oq0), "v"(oq1), "s"(lb), "s"(xo0), "s"(qo0));
    const char *pX, *pQ;
    asm volatile("s_waitcnt lgkmcnt(0)\n\ts_mov_b64 %0, %2\n\ts_mov_b64 %1, %3" : "=&s"(pX), "=&s"(pQ) : "s"(kx0), "s"(kq0));
    dma_block<0>(ox0, ox1, lb, pX + xo0);
    sfor<4>([&](auto T) { dma_block<0>(oq0, oq1, lb + (1 + T.value) * 8192, pQ + qo0 + T.value * 128); });
    sched0();
    const char *pK = p.in[br][2], *pV = p.in[br][3], *pO = p.in[br][4];
    char* ws = p.ws;
    const char* X = pX + (size_t)b * 64 * 896;
    const char* WQ = pQ + h * 512;
    const u32 ok0 = row * 512 + cA0, ok1 = (row + 8) * 512 + cA1 - 1024;
    const u32 oo0 = row * 896 + cW0, oo1 = (row + 8) * 896 + cW1 - 1024;
    const u32 op0 = L * 2048 + lane * 16;
    const char *KK, *VV, *WO, *PP;
    const u32 vrot = (bp + cg) & 3, krot = (hh & 1) * 2;

    u32 ep;
    auto issue = [&](auto I) {
      constexpr int i = I.value;
      if constexpr (i < 35) {
        constexpr int s = i / 5, t = i % 5;
        u32 ss = s + qrot; if (ss >= 7) ss -= 7;
        if constexpr (t == 0) dma_block<0>(ox0, ox1, lb + QG[s] * 8192, X + ss * 128);
        else dma_block<0>(oq0, oq1, lb + (QG[s] + t) * 8192, WQ + (size_t)ss * 64 * 8192 + (t - 1) * 128);
      } else if constexpr (i < 39) {
        const u32 kb = (i - 35) ^ krot;
        dma_block<0>(ok0, ok1, lb + (KS0 + kb) * 8192, KK + kb * 128);
      } else if constexpr (i < 71) {
        constexpr int u = (i - 39) / 4, t = u == 0 ? UO0[(i - 39) % 4] : (i - 39) % 4, j = u & 1;
        const u32 ss = ((u >> 1) + vrot) & 3;
        if constexpr (t < 2) dma_block<0>(ok0, ok1, lb + US[u][t] * 8192, VV + (size_t)(t * 8 + j) * 32768 + ss * 128);
        else dma_block<0>(oo0, oo1, lb + US[u][t] * 8192, WO + (size_t)((2 * j + t - 2) * 256 + ss * 64) * 896);
      } else {
        constexpr int t = i - 71;
        dma_block<1>(op0, op0, lb + ((t < 4 ? PS0 : PS1) + (t & 3)) * 8192, PP + (size_t)(((t >> 1) & 1) * 16 + (t >> 2) * 2 + (t & 1)) * 8192);
      }
    };
    auto issue_range = [&](auto A, auto B) { sfor<B.value - A.value>([&](auto J) { issue(IC(A.value + J.value)); }); };
    auto step = [&](auto K) {
      constexpr int k = K.value;
      issue_range(IC(ib(k - 1)), IC(ib(k)));
      wait_vm<open_at(k)>();
      bar();
    };

    issue_range(IC(5), IC(ib(0)));
    asm volatile("" : "+s"(pK), "+s"(pV), "+s"(pO), "+s"(ws));
    KK = pK + (size_t)(b * 8 + (h >> 1)) * 32768;
    VV = pV + (size_t)(bp * 16 + hg * 2) * 32768;
    WO = pO + (size_t)hg * 1024 * 896 + (vact ? cg : 0) * 128;
    PP = ws + WS_P + (size_t)((br * 8 + bp * 2) * 16 + hg * 4) * 8192;
    wait_vm<open_at(0)>();
    bar();
    sfor<4>([&](auto K) {
      constexpr int k = K.value + 1;
      if constexpr (k == KPF) {
        if (!vact) sfor<NPF>([&](auto J) { dma_word((((L >> 1) * 8 + (L & 1)) * 64 + J.value * 16) * 512 + lane * 128, lb + QG[k + 1] * 8192, VV); });
        issue_range(IC(ib(k - 1)), IC(ib(k)));
        if (!vact) wait_vm<open_at(k) + NPF>(); else wait_vm<open_at(k)>();
        bar();
      } else {
        step(IC(K.value + 1));
      }
    });
    ld_word_async(ep, 0u, ws + WS_EPOCH + (br * 4 + bp) * 256);
    step(IC(5));
    if (vact) {
      issue_range(IC(ib(5)), IC(ib(6)));
      const u32 tag = wait_tag<open_at(6)>(ep);
      bar();
      sfor<10>([&](auto K) { step(IC(K.value + 7)); });
      issue_range(IC(ib(16)), IC(79));
      __builtin_amdgcn_s_setprio(3);
      const u32 sgn = (u32)((int)tag >> 31);
      const bool neg = __builtin_amdgcn_readfirstlane(sgn) != 0;
      auto check = [&](auto H) {
        constexpr int hf = H.value;
        const u32 pv = opq(lds0 + op0 + (hf ? PS1 : PS0) * 8192);
        for (u32 spins = 0;; ++spins) {
          u32x4 v[8];
          sfor<4>([&](auto T) {
            if constexpr (hf) wait_vm<6 - 2 * T.value>(); else if constexpr (T.value == 0) wait_vm<8>();
            v[2 * T.value] = __builtin_bit_cast(u32x4, lds128(pv + T.value * 8192));
            v[2 * T.value + 1] = __builtin_bit_cast(u32x4, lds128(pv + T.value * 8192 + 1024));
          });
          u32 acc[2] = {sgn, sgn};
          if (neg) sfor<16>([&](auto I) { constexpr int i = I.value; acc[i & 1] = __builtin_amdgcn_bitop3_b32(acc[i & 1], v[i >> 1][2 * (i & 1)], v[i >> 1][2 * (i & 1) + 1], 0x80); });
          else sfor<16>([&](auto I) { constexpr int i = I.value; acc[i & 1] = __builtin_amdgcn_bitop3_b32(acc[i & 1], v[i >> 1][2 * (i & 1)], v[i >> 1][2 * (i & 1) + 1], 0xFE); });
          if (__all((((acc[0] ^ sgn) | (acc[1] ^ sgn)) & PTAG) == 0) || spins >= SPIN_CAP) break;
          issue_range(IC(71 + 4 * hf), IC(75 + 4 * hf));
          wait_vm<0>();
        }
      };
      check(IC(0));
      bar();
      check(IC(1));
      const char* FL = ws + WS_FLAG;
      const u32 fo = ((br * 4 + bp) * 4 + ((lane >> 2) & 3)) * 128 + (lane & 3) * 4;
      u32 fl;
      ld_word_async(fl, fo, FL);
      bar();
      if (!wait_all_eq<0>(fl, tag)) spin_all_eq(fo, FL, tag);
      bar();
    } else {
      issue_range(IC(ib(5)), IC(39));
      const u32 tag = wait_tag<8>(ep);
      bar();
      bar();
      wait_vm<0>();
      bar();
      char* ibx = ws + WS_X + (size_t)((br * 4 + bp) * 32 + hg) * 16384;
      const u32x4 e = {~0u, ~0u, ~0u, ~0u};
      const u32 mo = L * 4096 + lane * 16;
      sfor<3>([&](auto D) { st_x4_sc1(e, mo + D.value * 1024, ibx); });
      bar();
      for (u32 c2 = 1; c2 < 7; ++c2) sfor<3>([&](auto D) { st_x4_sc1(e, mo + D.value * 1024, ibx + (size_t)c2 * 65536); });
      wait_vm<0>();
      st_word_sc1(tag, L * 4, ws + WS_FLAG + ((br * 4 + bp) * 4 + hg) * 128);
    }
    return;
  }

  asm volatile("s_waitcnt lgkmcnt(0)" ::"s"(kx0), "s"(kq0));
  const u32 w = wv - 4;
  u32 cpf = 0;
  if (w == 0) cpf = *(const GAS u32*)((pc0 & ~127ull) + umin((idx % 5) * 4096 + lane * 64, CODE_LAST));
  if (w == 1) {
    const char* t = p.in[br][2] + (size_t)(b * 8 + (h >> 1)) * 32768;
    const u32 q = lane & 15;
    if (q == 1) t = p.in[br][3] + (size_t)(bp * 16 + hg * 2) * 32768;
    if (q == 2) t = p.in[br][3] + (size_t)(bp * 16 + 8 + hg * 2) * 32768;
    if (q == 3) t = p.in[br][4] + (size_t)hg * 1024 * 896;
    if (q == 4) t = p.in[br][4] + (size_t)(hg * 1024 + 1023) * 896;
    if (q == 5) t = p.ws + WS_P + (size_t)((br * 8 + b) * 16 + h) * 8192;
    if (q == 6) t = p.ws + WS_P + (size_t)((br * 8 + (b ^ 1)) * 16 + h) * 8192;
    if (q == 7) t = p.out + (size_t)bp * 128 * 1792;
    if (q == 8) t = p.out + (size_t)(bp * 128 + 127) * 1792;
    if (q >= 9) t = p.ws + WS_X + (size_t)((br * 4 + bp) * 8 + (vact ? cg : q - 9)) * 65536 + (q & 3) * 16384 + 3072;
    cpf = *(const GAS u32*)t;
  }
  char* xbase = p.ws + WS_X + (size_t)((br * 4 + bp) * 8 + cg) * 65536;
  char* pbase = p.ws + WS_P + (size_t)((br * 8 + b) * 16 + h) * 8192;
  char* obase = p.out;
  char* ebase = p.ws + WS_EPOCH + (br * 4 + bp) * 256;
  asm volatile("" : "+s"(xbase), "+s"(pbase), "+s"(obase), "+s"(ebase));
  const u32 epoch = __builtin_amdgcn_raw_buffer_load_b32(__builtin_amdgcn_make_buffer_rsrc(ebase, 0, 4, 0x00020000), (int)opq(0), 0, 16);
  __amdgpu_buffer_rsrc_t rx = __builtin_amdgcn_make_buffer_rsrc(xbase, 0, 65536, 0x00020000);
  const int xo = (int)opq(w * 4096 + lane * 16 + hg * 16384);

  const u32 i16 = lane & 15, g = lane >> 4, q4 = i16 >> 2, pp = lane & 3;
  const u32 a16 = lds0 + i16 * 128 + ((g ^ (i16 >> 1)) * 16);
  const u32 aT = lds0 + (8 * g + q4) * 128 + (((pp >> 1) ^ ((q4 & 2) | ((g & 1) << 2))) * 16) + 8 * (pp & 1);
  const u32 bb = w >> 1, ct = w & 1;
  u32 wa[2][3], va[2][2], kr[4];
  sfor<2>([&](auto C) {
    sfor<3>([&](auto R) { wa[C.value][R.value] = opq((aT ^ (64 * ct + 32 * C.value)) + R.value * 65536); });
    sfor<2>([&](auto R) { va[R.value][C.value] = opq((a16 ^ (64 * C.value)) + bb * 8192 + R.value * 81920); });
  });
  const u32 pown = opq(lds0 + lane * 16 + (PS1 + 2 * bb) * 8192 + hg * 2048);
  const u32 aK = lds0 + KS0 * 8192 + i16 * 128 + (((g >> 1) ^ (i16 >> 1)) * 16) + (g & 1) * 8;
  sfor<4>([&](auto I) { kr[I.value] = opq(aK ^ (32 * I.value)); });
  const u32 qwr = opq(lds0 + QX0 * 8192 + w * 2048 + lane * 16), qrd = opq(lds0 + (QX0 + w) * 8192 + lane * 16);
  __amdgpu_buffer_rsrc_t rp = __builtin_amdgcn_make_buffer_rsrc(pbase, 0, 8192, 0x00020000);
  const int po = (int)opq(w * 2048 + lane * 16);
  __amdgpu_buffer_rsrc_t ro = __builtin_amdgcn_make_buffer_rsrc(obase, 0, 512 * 1792, 0x00020000);
  const int oo = (int)opq(((2 * bp + bb) * 64 + 16 * hg + i16) * 1792 + (br * 448 + 64 * cg + 32 * ct + 16 * (g & 1) + 8 * (g >> 1)) * 2);

  auto pk44 = [](const f32x4& a, const f32x4& c) { return u32x4{pk2(a[0], a[1]), pk2(a[2], a[3]), pk2(c[0], c[1]), pk2(c[2], c[3])}; };
  auto weave = []() { sfor<12>([&](auto) { sgb<SG_DSR, 1>(); sgb<SG_MFMA, 1>(); }); sgb<SG_MFMA, 4>(); };
  s16x8 A[2][4], B[2][4];
  f32x4 sa[4] = {};
  {
    f32x4 qa[4][4] = {};
    u32 tq[2][4], xr[2][2];
    sfor<2>([&](auto H) {
      sfor<4>([&](auto C) { tq[H.value][C.value] = opq((aT ^ (32 * C.value)) + (w + 1) * 8192 + H.value * 81920); });
      sfor<2>([&](auto K) { xr[H.value][K.value] = opq((a16 ^ (64 * K.value)) + H.value * 81920); });
    });
    auto ld = [&](auto G) {
      constexpr int gs = G.value, s = gs >> 1, ks = gs & 1, hi = QG[s] >= 10, o = (QG[s] - 10 * hi) * 8192;
      sfor<4>([&](auto I) {
        A[ks][I.value] = ldstr(tq[hi][I.value] + o + ks * 4096);
        B[ks][I.value] = lds128(xr[hi][ks] + o + I.value * 2048);
      });
    };
    bar();
    __builtin_amdgcn_s_setprio(3);
    ld(IC(0));
    sfor<13>([&](auto G) {
      constexpr int gs = G.value, bf = gs & 1;
      sched0();
      if constexpr (bf == 1) { bar(); sched0(); }
      ld(IC(gs + 1));
      sfor<16>([&](auto I) { constexpr int d = I.value >> 2, q = I.value & 3; qa[d][q] = mfma16(A[bf][d], B[bf][q], qa[d][q]); });
      weave();
    });
    sched0(); bar_lds(); sched0();
    auto wrq = [&](auto I) {
      constexpr int m = I.value & 1, q = I.value >> 1;
      *(LAS u32x4*)(size_t)(qwr + q * 8192 + m * 1024) = pk44(qa[2 * m][q], qa[2 * m + 1][q]);
    };
    sfor<8>([&](auto I) {
      constexpr int m = I.value & 1, q = I.value >> 1;
      qa[2 * m][q] = mfma16(A[1][2 * m], B[1][q], qa[2 * m][q]);
      qa[2 * m + 1][q] = mfma16(A[1][2 * m + 1], B[1][q], qa[2 * m + 1][q]);
      sched0();
      if constexpr (I.value >= 2) wrq(IC(I.value - 2));
      sched0();
    });
    wrq(IC(6)); wrq(IC(7));
    sched0(); bar_lds(); sched0();
    s16x8 KF[8], QF[8];
    auto ldk = [&](auto F) {
      constexpr int f = F.value, u = f >> 2, o = (u >> 1) * 8192 + (f & 3) * 2048;
      if constexpr ((f & 3) == 0) QF[u] = lds128(qrd + u * 1024);
      s16x4 lo = *(const LAS s16x4*)(size_t)(kr[2 * (u & 1)] + o), hi = *(const LAS s16x4*)(size_t)(kr[2 * (u & 1) + 1] + o);
      KF[f & 7] = __builtin_shufflevector(lo, hi, 0, 1, 2, 3, 4, 5, 6, 7);
    };
    sfor<6>([&](auto F) { ldk(F); sched0(); });
    sfor<32>([&](auto F) {
      constexpr int f = F.value;
      sched0();
      if constexpr (f + 6 < 32) ldk(IC(f + 6));
      sa[f & 3] = mfma16(KF[f & 7], QF[f >> 2], sa[f & 3]);
    });
  }
  asm volatile("s_nop 7\n\ts_nop 3" : "+v"(sa[0]), "+v"(sa[1]), "+v"(sa[2]), "+v"(sa[3]));

  u32 ptg;
  {
    const float cs = 0.0625f * 1.44269504088896340736f;
    float m = vmax3(vmax3(sa[0][0], sa[0][1], sa[0][2]), vmax3(sa[0][3], sa[1][0], sa[1][1]), vmax3(sa[1][2], sa[1][3], sa[2][0]));
    m = vmax3(m, vmax3(sa[2][1], sa[2][2], sa[2][3]), vmax3(sa[3][0], sa[3][1], vmax(sa[3][2], sa[3][3])));
    m = xmax(m) * cs;
    ptg = (u32)((int)~epoch >> 31) & PTAG;
    asm volatile("" ::"v"(cpf));
    float e[16];
    sfor<16>([&](auto I) { constexpr int i = I.value; e[i] = __builtin_amdgcn_exp2f(fmaf(sa[i >> 2][i & 3], cs, -m)); });
    const float sum = (((e[0] + e[1]) + (e[2] + e[3])) + ((e[4] + e[5]) + (e[6] + e[7]))) + (((e[8] + e[9]) + (e[10] + e[11])) + ((e[12] + e[13]) + (e[14] + e[15])));
    const float r = __builtin_amdgcn_rcpf(xsum(sum));
    sfor<16>([&](auto I) { e[I.value] *= r; });
    sched0();
    u32x4 pf[2];
    sfor<8>([&](auto I) { pf[I.value >> 2][I.value & 3] = pk2(e[2 * I.value], e[2 * I.value + 1]); });
    sched0();
    sfor<8>([&](auto I) { pf[I.value >> 2][I.value & 3] = (pf[I.value >> 2][I.value & 3] & ~PTAG) | ptg; });
    __builtin_amdgcn_raw_buffer_store_b128(pf[0], rp, po, 0, 16);
    __builtin_amdgcn_raw_buffer_store_b128(pf[1], rp, po + 1024, 0, 16);
  }
  sched0(); bar(); sched0();
  if (!vact) return;

  f32x4 xa[4][4][2] = {};
  s16x8 PF[4], XF[16];
  auto ldv = [&](auto G) {
    constexpr int gv = G.value, u = gv >> 1, ks = gv & 1, hi = US[u][0] >= 10, vo = (US[u][0] - 10 * hi) * 8192;
    sfor<4>([&](auto I) {
      constexpr int sl = US[u][2 + (I.value >> 1)];
      A[ks][I.value] = lds128(va[hi][ks] + vo + I.value * 2048);
      B[ks][I.value] = ldstr(wa[I.value & 1][sl >> 3] + (sl & 7) * 8192 + ks * 4096);
    });
  };
  auto cvx = [&](auto I) {
    constexpr int i = I.value;
    u32x4 t = pk44(xa[i >> 2][i & 2][i & 1], xa[i >> 2][(i & 2) + 1][i & 1]);
    sfor<4>([&](auto E) { t[E.value] ^= ptg; });
    XF[i] = __builtin_bit_cast(s16x8, t);
  };
  const u32 pa[4] = {opq(pown ^ 2048), opq(pown ^ 4096), opq(pown ^ 6144), pown};
  auto ldp = [&](auto F) {
    constexpr int f = F.value, hd = fin_head(f);
    PF[f & 3] = lds128(pa[fin_part(f)] + ((hd & 1) + (hd < 2 ? PS0 - PS1 : 0)) * 8192 + (f & 1) * 1024);
  };
  ldv(IC(0));
  sfor<16>([&](auto G) {
    constexpr int gv = G.value, bf = gv & 1, j = (gv >> 1) & 1;
    sched0();
    if constexpr (bf == 1 && gv < 15) { bar(); sched0(); }
    if constexpr (gv < 15) ldv(IC(gv + 1));
    if constexpr (gv >= 14) sfor<4>([&](auto I) { cvx(IC(4 * (gv - 14) + I.value)); });
    sfor<16>([&](auto I) {
      constexpr int e2 = I.value >> 3, kt = (I.value >> 1) & 3, c2 = I.value & 1;
      xa[2 * j + e2][kt][c2] = mfma16(A[bf][kt], B[bf][2 * e2 + c2], xa[2 * j + e2][kt][c2]);
    });
    if constexpr (gv == 14) { sfor<12>([&](auto) { sgb<SG_DSR, 1>(); sgb<SG_MFMA, 1>(); sgb<SG_VALU, 2>(); }); sfor<4>([&](auto) { sgb<SG_MFMA, 1>(); sgb<SG_VALU, 2>(); }); }
    else if constexpr (gv == 15) { sfor<16>([&](auto) { sgb<SG_MFMA, 1>(); sgb<SG_VALU, 2>(); }); }
    else weave();
  });
  {
    sched0(); bar();
    sfor<4>([&](auto F) { ldp(F); });
    sched0();
    sfor<NCV0>([&](auto I) { cvx(IC(8 + I.value)); pin(XF[8 + I.value]); });
    f32x4 oa[4][2] = {};
    auto send = [&](auto D) {
      constexpr int d = D.value;
      u32x4 a = pk44(oa[d - 1][0], oa[d - 1][1]);
      sfor<4>([&](auto E) { a[E.value] = umin(a[E.value], 0xFFFFFFFEu); });
      __builtin_amdgcn_raw_buffer_store_b128(a, rx, (xo ^ (d * 16384)) + (d - 1) * 1024, 0, 16);
    };
    sfor<32>([&](auto F) {
      constexpr int f = F.value, part = fin_part(f), x = 4 * fin_head(f) + 2 * (f & 1);
      sched0();
      if constexpr (f == 8 || f == 17) { bar(); sched0(); }
      const s16x8 pf = PF[f & 3];
      if constexpr (f + 4 < 32) ldp(IC(f + 4));
      oa[part][0] = mfma16(XF[x], pf, oa[part][0]);
      if constexpr (f + NCV0 < 8) { cvx(IC(8 + NCV0 + f)); sgb<SG_DSR, 1>(); sgb<SG_MFMA, 1>(); sgb<SG_VALU, 4>(); sgb<SG_MFMA, 1>(); sgb<SG_VALU, 4>(); }
      oa[part][1] = mfma16(XF[x + 1], pf, oa[part][1]);
      if constexpr (f == 17 || f == 21 || f == 25) send(IC((f - 13) / 4));
    });
    asm volatile("" : "+v"(oa[3][0]), "+v"(oa[3][1]));
    __builtin_amdgcn_s_sleep(4);
    u32x4 v[3];
    auto fetch = [&]() { sfor<3>([&](auto D) { v[D.value] = __builtin_amdgcn_raw_buffer_load_b128(rx, xo + D.value * 1024, 0, 16); }); };
    fetch();
#pragma nounroll
    for (u32 spins = 0; spins < SPIN_CAP; ++spins) {
      u32 mx = 0;
      sfor<3>([&](auto D) { sfor<4>([&](auto E) { mx = umax(mx, v[D.value][E.value]); }); });
      if (__all(mx != ~0u)) break;
      asm volatile("" ::: "memory");
      fetch();
    }
    float o[8];
    sfor<8>([&](auto I) {
      constexpr int i = I.value;
      auto up = [&](u32 x) { return __uint_as_float((i & 1) ? (x & 0xFFFF0000u) : (x << 16)); };
      o[i] = ((oa[3][i >> 2][i & 3] + up(v[0][i >> 1])) + up(v[1][i >> 1])) + up(v[2][i >> 1]);
    });
    const auto s0 = __builtin_amdgcn_permlane16_swap(pk2(o[0], o[1]), pk2(o[4], o[5]), false, false);
    const auto s1 = __builtin_amdgcn_permlane16_swap(pk2(o[2], o[3]), pk2(o[6], o[7]), false, false);
    const u32 w0 = s0[0], w1 = s1[0], w2 = s0[1], w3 = s1[1];
    __builtin_amdgcn_raw_buffer_store_b128(u32x4{w0, w1, w2, w3}, ro, oo, 0, 0);
    if (hg == 0 && cg == 0 && threadIdx.x == 256)
      __hip_atomic_fetch_xor((GAS u32*)ebase, PTAG, __ATOMIC_RELAXED, __HIP_MEMORY_SCOPE_AGENT);
  }
}

extern "C" void kernel_launch(void* const* d_in, const int* in_sizes, int n_in, void* d_out, int out_size, void* d_ws,
                              size_t ws_size, hipStream_t stream) {
  Params p;
  for (int br = 0; br < 2; br++) {
    p.in[br][0] = (const char*)d_in[br * 5 + 0];
    p.in[br][1] = (const char*)d_in[br * 5 + 3];
    p.in[br][2] = (const char*)d_in[br * 5 + 1];
    p.in[br][3] = (const char*)d_in[br * 5 + 2];
    p.in[br][4] = (const char*)d_in[br * 5 + 4];
  }
  p.out = (char*)d_out;
  p.ws = (char*)d_ws;
  (void)hipFuncSetAttribute((const void*)fused_gqa2, hipFuncAttributeMaxDynamicSharedMemorySize, LDS_BYTES);
  fused_gqa2<<<256, 512, LDS_BYTES, stream>>>(p);
}
```

```cpp
#include <hip/hip_runtime.h>
#include <utility>

typedef unsigned int u32;
typedef unsigned long long u64;
typedef short s16x4 __attribute__((ext_vector_type(4)));
typedef short s16x8 __attribute__((ext_vector_type(8)));
typedef float f32x2 __attribute__((ext_vector_type(2)));
typedef float f32x4 __attribute__((ext_vector_type(4)));
typedef u32 u32x4 __attribute__((ext_vector_type(4)));
typedef __bf16 bf16x2 __attribute__((ext_vector_type(2)));
#define LAS __attribute__((address_space(3)))
#define GAS __attribute__((address_space(1)))
#define DEV __device__ __forceinline__
#define IC(n) std::integral_constant<int, n>{}

constexpr size_t WS_EPOCH = 0;
constexpr size_t WS_FLAG = 4096;
constexpr size_t WS_P = 65536;
constexpr size_t WS_X = WS_P + 256 * 8192;
constexpr size_t WS_STAMP = WS_X + 256 * 16384;
constexpr int LDS_BYTES = 163840;
constexpr u32 SPIN_CAP = 1u << 20;
constexpr u32 CODE_LAST = 18432;
constexpr u32 PTAG = 0x80008000u;
constexpr int NPF = 2, KPF = 3;
constexpr int NCV0 = 4;

struct Params {
  const char* in[2][5];
  char* out;
  char* ws;
};

template <class F, int... Is> DEV void sfor_impl(F&& f, std::integer_sequence<int, Is...>) { (f(std::integral_constant<int, Is>{}), ...); }
template <int N, class F> DEV void sfor(F&& f) { sfor_impl(f, std::make_integer_sequence<int, N>{}); }

template <int N> DEV void wait_vm() { asm volatile("s_waitcnt vmcnt(%0)" ::"n"(N) : "memory"); }
DEV void bar() { asm volatile("s_barrier" ::: "memory"); }
DEV void bar_lds() { asm volatile("s_waitcnt lgkmcnt(0)\n\ts_barrier" ::: "memory"); }
DEV void sched0() { __builtin_amdgcn_sched_barrier(0); }
DEV void pin(s16x8& x) { asm volatile("" : "+v"(x)); }
DEV u32 opq(u32 x) { asm volatile("" : "+v"(x)); return x; }
template <int M, int N> DEV void sgb() { __builtin_amdgcn_sched_group_barrier(M, N, 0); }
constexpr int SG_VALU = 0x2, SG_MFMA = 0x8, SG_DSR = 0x100;

template <int SC1> DEV void dma_block(u32 v0, u32 v1, u32 lds, const char* base) {
  if constexpr (SC1 == 0)
    asm volatile("s_mov_b32 m0, %2\n\ts_nop 0\n\tglobal_load_lds_dwordx4 %0, %3\n\tglobal_load_lds_dwordx4 %1, %3 offset:1024"
                 ::"v"(v0), "v"(v1), "s"(lds), "s"(base) : "memory");
  else
    asm volatile("s_mov_b32 m0, %2\n\ts_nop 0\n\tglobal_load_lds_dwordx4 %0, %3 sc1\n\tglobal_load_lds_dwordx4 %1, %3 offset:1024 sc1"
                 ::"v"(v0), "v"(v1), "s"(lds), "s"(base) : "memory");
}
DEV void dma_word(u32 v, u32 lds, const char* base) {
  asm volatile("s_mov_b32 m0, %1\n\ts_nop 0\n\tglobal_load_lds_dword %0, %2" ::"v"(v), "s"(lds), "s"(base) : "memory");
}
DEV void ld_word_async(u32& d, u32 off, const char* base) { asm volatile("global_load_dword %0, %1, %2 sc1" : "=v"(d) : "v"(off), "s"(base) : "memory"); }
DEV void st_x4_sc1(u32x4 v, u32 off, char* base) { asm volatile("global_store_dwordx4 %0, %1, %2 sc1\n\ts_nop 1" ::"v"(off), "v"(v), "s"(base) : "memory"); }
DEV void st_word_sc1(u32 v, u32 off, char* base) { asm volatile("global_store_dword %0, %1, %2 sc1" ::"v"(off), "v"(v), "s"(base) : "memory"); }
template <int N> DEV bool wait_all_eq(u32 d, u32 want) {
  u64 m;
  asm volatile("s_waitcnt vmcnt(%3)\n\tv_cmp_ne_u32 %0, %1, %2" : "=s"(m) : "v"(d), "v"(want), "n"(N) : "memory");
  return m == 0;
}
template <int N> DEV u32 wait_tag(u32 d) { u32 r; asm volatile("s_waitcnt vmcnt(%2)\n\tv_xor_b32 %0, 0x80008000, %1" : "=v"(r) : "v"(d), "n"(N) : "memory"); return r; }
DEV void spin_all_eq(u32 off, const char* base, u32 want) {
  for (u32 spins = 0; spins < SPIN_CAP; ++spins) {
    u64 m; u32 d;
    asm volatile("global_load_dword %1, %2, %3 sc1\n\ts_waitcnt vmcnt(0)\n\tv_cmp_ne_u32 %0, %1, %4" : "=s"(m), "=&v"(d) : "v"(off), "s"(base), "v"(want) : "memory");
    if (m == 0) break;
  }
}

DEV s16x8 lds128(u32 a) { return *(const LAS s16x8*)(size_t)a; }
DEV s16x8 ldstr(u32 a) {
  s16x4 lo = __builtin_amdgcn_ds_read_tr16_b64_v4i16((LAS s16x4*)(size_t)a);
  s16x4 hi = __builtin_amdgcn_ds_read_tr16_b64_v4i16((LAS s16x4*)(size_t)(a + 512));
  return __builtin_shufflevector(lo, hi, 0, 1, 2, 3, 4, 5, 6, 7);
}
DEV u32 pk2(float a, float b) { f32x2 t = {a, b}; return __builtin_bit_cast(u32, __builtin_convertvector(t, bf16x2)); }
DEV f32x4 mfma16(s16x8 a, s16x8 b, f32x4 c) { return __builtin_amdgcn_mfma_f32_16x16x32_bf16(a, b, c, 0, 0, 0); }
DEV float vmax(float a, float b) { float r; asm("v_max_f32 %0, %1, %2" : "=v"(r) : "v"(a), "v"(b)); return r; }
DEV float vmax3(float a, float b, float c) { float r; asm("v_max3_f32 %0, %1, %2, %3" : "=v"(r) : "v"(a), "v"(b), "v"(c)); return r; }
DEV float xmax(float m) {
  u32 u = __float_as_uint(m);
  auto s = __builtin_amdgcn_permlane32_swap(u, u, false, false);
  u32 a = s[0], b = s[1];
  m = vmax(__uint_as_float(a), __uint_as_float(b));
  u = __float_as_uint(m);
  auto t = __builtin_amdgcn_permlane16_swap(u, u, false, false);
  a = t[0]; b = t[1];
  return vmax(__uint_as_float(a), __uint_as_float(b));
}
DEV float xsum(float m) {
  u32 u = __float_as_uint(m);
  auto s = __builtin_amdgcn_permlane32_swap(u, u, false, false);
  u32 a = s[0], b = s[1];
  m = __uint_as_float(a) + __uint_as_float(b);
  u = __float_as_uint(m);
  auto t = __builtin_amdgcn_permlane16_swap(u, u, false, false);
  a = t[0]; b = t[1];
  return __uint_as_float(a) + __uint_as_float(b);
}
DEV u64 rtnow() { u64 t; asm volatile("s_memrealtime %0\n\ts_waitcnt lgkmcnt(0)" : "=s"(t)::"memory"); return t; }
DEV u32 umin(u32 a, u32 b) { return a < b ? a : b; }
DEV u32 umax(u32 a, u32 b) { return a > b ? a : b; }

constexpr int QG[7] = {0, 5, 10, 15, 0, 5, 10};
constexpr int KS0 = 15, QX0 = 10;
constexpr int US[8][4] = {{0, 1, 19, 2}, {3, 4, 5, 6}, {7, 8, 9, 14}, {15, 16, 17, 18}, {10, 11, 12, 13}, {0, 1, 19, 2}, {3, 4, 5, 6}, {7, 8, 9, 14}};
constexpr int UO0[4] = {2, 0, 1, 3};
constexpr int PS0 = 15, PS1 = 10;
constexpr int imin(int a, int b) { return a < b ? a : b; }
constexpr int imax(int a, int b) { return a > b ? a : b; }
constexpr int NEED[17] = {5, 10, 15, 20, 25, 30, 35, 35, 39, 43, 47, 51, 55, 59, 63, 67, 71};
constexpr int ALLOW[17] = {20, 20, 20, 25, 30, 35, 40, 45, 51, 51, 59, 59, 63, 67, 71, 75, 79};
constexpr int LOOK[17] = {5, 5, 5, 5, 5, 5, 5, 7, 7, 6, 6, 6, 6, 6, 6, 6, 6};
constexpr int ib(int k) { return k < 0 ? 0 : imax(ib(k - 1), imin(ALLOW[k], NEED[k] + LOOK[k])); }
constexpr int open_at(int k) { return 2 * (ib(k) - NEED[k]) + (k > 4 && NEED[k] <= ib(4) ? 1 : 0); }
static_assert(ib(4) < NEED[6] && ib(5) == 35 && ib(KPF - 1) == 5 * (KPF + 1), "");
constexpr int fin_part(int f) { return f < 12 ? f >> 2 : f < 24 ? (f - 12) >> 2 : 3; }
constexpr int fin_head(int f) { return f < 12 ? (f >> 1) & 1 : f < 24 ? 2 + ((f >> 1) & 1) : (f - 24) >> 1; }

__global__ __launch_bounds__(512, 2) void fused_gqa2(Params p) {
  extern __shared__ __attribute__((aligned(16))) char smem[];
  u64 pc0;
  asm volatile("s_getpc_b64 %0" : "=s"(pc0));
  u64 kx0, kq0;
  asm volatile("s_load_dwordx2 %0, %2, %3\n\ts_load_dwordx2 %1, %2, %3 offset:8" : "=&s"(kx0), "=&s"(kq0)
               : "s"((u64)__builtin_amdgcn_kernarg_segment_ptr()), "s"(((blockIdx.x >> 2) & 1) * 40));
  const u32 bid = blockIdx.x, xcd = bid & 7, idx = bid >> 3;
  const u32 br = xcd >> 2, hg = xcd & 3, b = idx & 7, hh = (idx >> 3) & 3, h = hg * 4 + hh;
  const u32 bp = b >> 1, cg = 2 * hh + (b & 1);
  const bool vact = cg < 7;
  const u32 lane = threadIdx.x & 63;
  const u32 wv = __builtin_amdgcn_readfirstlane(threadIdx.x >> 6);
  const u32 lds0 = (u32)(size_t)(LAS char*)smem;

  const u64 rt0 = __builtin_amdgcn_s_memrealtime();
  if (__builtin_expect(wv < 4, 1)) {
    const u32 L = wv;
    __builtin_amdgcn_s_setprio(2);
    const u32 r8 = lane >> 3, c = lane & 7, row = 16 * L + r8;
    const u32 cA0 = (c ^ (r8 >> 1)) * 16, cA1 = (c ^ (4 + (r8 >> 1))) * 16, cW0 = (c ^ (r8 & 2)) * 16, cW1 = (c ^ ((r8 & 2) | 4)) * 16;
    const u32 ox0 = row * 896 + cA0, ox1 = (row + 8) * 896 + cA1 - 1024;
    const u32 oq0 = row * 8192 + cW0, oq1 = (row + 8) * 8192 + cW1 - 1024;
    const u32 lb = lds0 + L * 2048;
    const u32 qrot = ((b + hh) & 3) * 7 / 4;
    const u32 xo0 = b * 64 * 896 + qrot * 128, qo0 = h * 512 + qrot * 64 * 8192;
    asm volatile("" ::"v"(ox0), "v"(ox1), "v"(oq0), "v"(oq1), "s"(lb), "s"(xo0), "s"(qo0));
    const char *pX, *pQ;
    asm volatile("s_waitcnt lgkmcnt(0)\n\ts_mov_b64 %0, %2\n\ts_mov_b64 %1, %3" : "=&s"(pX), "=&s"(pQ) : "s"(kx0), "s"(kq0));
    dma_block<0>(ox0, ox1, lb, pX + xo0);
    sfor<4>([&](auto T) { dma_block<0>(oq0, oq1, lb + (1 + T.value) * 8192, pQ + qo0 + T.value * 128); });
    sched0();
    const char *pK = p.in[br][2], *pV = p.in[br][3], *pO = p.in[br][4];
    char* ws = p.ws;
    const char* X = pX + (size_t)b * 64 * 896;
    const char* WQ = pQ + h * 512;
    const u32 ok0 = row * 512 + cA0, ok1 = (row + 8) * 512 + cA1 - 1024;
    const u32 oo0 = row * 896 + cW0, oo1 = (row + 8) * 896 + cW1 - 1024;
    const u32 op0 = L * 2048 + lane * 16;
    const char *KK, *VV, *WO, *PP;
    const u32 vrot = (bp + cg) & 3, krot = (hh & 1) * 2;

    u32 ep;
    auto issue = [&](auto I) {
      constexpr int i = I.value;
      if constexpr (i < 35) {
        constexpr int s = i / 5, t = i % 5;
        u32 ss = s + qrot; if (ss >= 7) ss -= 7;
        if constexpr (t == 0) dma_block<0>(ox0, ox1, lb + QG[s] * 8192, X + ss * 128);
        else dma_block<0>(oq0, oq1, lb + (QG[s] + t) * 8192, WQ + (size_t)ss * 64 * 8192 + (t - 1) * 128);
      } else if constexpr (i < 39) {
        const u32 kb = (i - 35) ^ krot;
        dma_block<0>(ok0, ok1, lb + (KS0 + kb) * 8192, KK + kb * 128);
      } else if constexpr (i < 71) {
        constexpr int u = (i - 39) / 4, t = u == 0 ? UO0[(i - 39) % 4] : (i - 39) % 4, j = u & 1;
        const u32 ss = ((u >> 1) + vrot) & 3;
        if constexpr (t < 2) dma_block<0>(ok0, ok1, lb + US[u][t] * 8192, VV + (size_t)(t * 8 + j) * 32768 + ss * 128);
        else dma_block<0>(oo0, oo1, lb + US[u][t] * 8192, WO + (size_t)((2 * j + t - 2) * 256 + ss * 64) * 896);
      } else {
        constexpr int t = i - 71;
        dma_block<1>(op0, op0, lb + ((t < 4 ? PS0 : PS1) + (t & 3)) * 8192, PP + (size_t)(((t >> 1) & 1) * 16 + (t >> 2) * 2 + (t & 1)) * 8192);
      }
    };
    auto issue_range = [&](auto A, auto B) { sfor<B.value - A.value>([&](auto J) { issue(IC(A.value + J.value)); }); };
    auto step = [&](auto K) {
      constexpr int k = K.value;
      issue_range(IC(ib(k - 1)), IC(ib(k)));
      wait_vm<open_at(k)>();
      bar();
    };

    issue_range(IC(5), IC(ib(0)));
    asm volatile("" : "+s"(pK), "+s"(pV), "+s"(pO), "+s"(ws));
    KK = pK + (size_t)(b * 8 + (h >> 1)) * 32768;
    VV = pV + (size_t)(bp * 16 + hg * 2) * 32768;
    WO = pO + (size_t)hg * 1024 * 896 + (vact ? cg : 0) * 128;
    PP = ws + WS_P + (size_t)((br * 8 + bp * 2) * 16 + hg * 4) * 8192;
    wait_vm<open_at(0)>();
    bar();
    sfor<4>([&](auto K) {
      constexpr int k = K.value + 1;
      if constexpr (k == KPF) {
        if (!vact) sfor<NPF>([&](auto J) { dma_word((((L >> 1) * 8 + (L & 1)) * 64 + J.value * 16) * 512 + lane * 128, lb + QG[k + 1] * 8192, VV); });
        issue_range(IC(ib(k - 1)), IC(ib(k)));
        if (!vact) wait_vm<open_at(k) + NPF>(); else wait_vm<open_at(k)>();
        bar();
      } else {
        step(IC(K.value + 1));
      }
    });
    ld_word_async(ep, 0u, ws + WS_EPOCH + (br * 4 + bp) * 256);
    step(IC(5));
    if (vact) {
      issue_range(IC(ib(5)), IC(ib(6)));
      const u32 tag = wait_tag<open_at(6)>(ep);
      bar();
      sfor<10>([&](auto K) { step(IC(K.value + 7)); });
      issue_range(IC(ib(16)), IC(79));
      __builtin_amdgcn_s_setprio(3);
      const u32 sgn = (u32)((int)tag >> 31);
      const bool neg = __builtin_amdgcn_readfirstlane(sgn) != 0;
      auto check = [&](auto H) {
        constexpr int hf = H.value;
        const u32 pv = opq(lds0 + op0 + (hf ? PS1 : PS0) * 8192);
        for (u32 spins = 0;; ++spins) {
          u32x4 v[8];
          sfor<4>([&](auto T) {
            if constexpr (hf) wait_vm<6 - 2 * T.value>(); else if constexpr (T.value == 0) wait_vm<8>();
            v[2 * T.value] = __builtin_bit_cast(u32x4, lds128(pv + T.value * 8192));
            v[2 * T.value + 1] = __builtin_bit_cast(u32x4, lds128(pv + T.value * 8192 + 1024));
          });
          u32 acc[2] = {sgn, sgn};
          if (neg) sfor<16>([&](auto I) { constexpr int i = I.value; acc[i & 1] = __builtin_amdgcn_bitop3_b32(acc[i & 1], v[i >> 1][2 * (i & 1)], v[i >> 1][2 * (i & 1) + 1], 0x80); });
          else sfor<16>([&](auto I) { constexpr int i = I.value; acc[i & 1] = __builtin_amdgcn_bitop3_b32(acc[i & 1], v[i >> 1][2 * (i & 1)], v[i >> 1][2 * (i & 1) + 1], 0xFE); });
          if (__all((((acc[0] ^ sgn) | (acc[1] ^ sgn)) & PTAG) == 0) || spins >= SPIN_CAP) break;
          issue_range(IC(71 + 4 * hf), IC(75 + 4 * hf));
          wait_vm<0>();
        }
      };
      check(IC(0));
      bar();
      check(IC(1));
      const char* FL = ws + WS_FLAG;
      const u32 fo = ((br * 4 + bp) * 4 + ((lane >> 2) & 3)) * 128 + (lane & 3) * 4;
      u32 fl;
      ld_word_async(fl, fo, FL);
      bar();
      if (!wait_all_eq<0>(fl, tag)) spin_all_eq(fo, FL, tag);
      bar();
      if (threadIdx.x == 0) { u64* st = (u64*)(ws + WS_STAMP) + bid * 8; st[2] = rtnow(); }
    } else {
      issue_range(IC(ib(5)), IC(39));
      const u32 tag = wait_tag<8>(ep);
      bar();
      bar();
      wait_vm<0>();
      bar();
      char* ibx = ws + WS_X + (size_t)((br * 4 + bp) * 32 + hg) * 16384;
      const u32x4 e = {~0u, ~0u, ~0u, ~0u};
      const u32 mo = L * 4096 + lane * 16;
      sfor<3>([&](auto D) { st_x4_sc1(e, mo + D.value * 1024, ibx); });
      bar();
      for (u32 c2 = 1; c2 < 7; ++c2) sfor<3>([&](auto D) { st_x4_sc1(e, mo + D.value * 1024, ibx + (size_t)c2 * 65536); });
      wait_vm<0>();
      if (threadIdx.x == 192) { u64* st = (u64*)(ws + WS_STAMP) + bid * 8; st[2] = rtnow(); }
      st_word_sc1(tag, L * 4, ws + WS_FLAG + ((br * 4 + bp) * 4 + hg) * 128);
    }
    return;
  }

  asm volatile("s_waitcnt lgkmcnt(0)" ::"s"(kx0), "s"(kq0));
  const u32 w = wv - 4;
  u32 cpf = 0;
  if (w == 0) cpf = *(const GAS u32*)((pc0 & ~127ull) + umin((idx % 5) * 4096 + lane * 64, CODE_LAST));
  char* xbase = p.ws + WS_X + (size_t)((br * 4 + bp) * 8 + cg) * 65536;
  char* pbase = p.ws + WS_P + (size_t)((br * 8 + b) * 16 + h) * 8192;
  char* obase = p.out;
  char* ebase = p.ws + WS_EPOCH + (br * 4 + bp) * 256;
  asm volatile("" : "+s"(xbase), "+s"(pbase), "+s"(obase), "+s"(ebase));
  const u32 epoch = __builtin_amdgcn_raw_buffer_load_b32(__builtin_amdgcn_make_buffer_rsrc(ebase, 0, 4, 0x00020000), (int)opq(0), 0, 16);
  __amdgpu_buffer_rsrc_t rx = __builtin_amdgcn_make_buffer_rsrc(xbase, 0, 65536, 0x00020000);
  const int xo = (int)opq(w * 4096 + lane * 16 + hg * 16384);

  const u32 i16 = lane & 15, g = lane >> 4, q4 = i16 >> 2, pp = lane & 3;
  const u32 a16 = lds0 + i16 * 128 + ((g ^ (i16 >> 1)) * 16);
  const u32 aT = lds0 + (8 * g + q4) * 128 + (((pp >> 1) ^ ((q4 & 2) | ((g & 1) << 2))) * 16) + 8 * (pp & 1);
  const u32 bb = w >> 1, ct = w & 1;
  u32 wa[2][3], va[2][2], kr[4];
  sfor<2>([&](auto C) {
    sfor<3>([&](auto R) { wa[C.value][R.value] = opq((aT ^ (64 * ct + 32 * C.value)) + R.value * 65536); });
    sfor<2>([&](auto R) { va[R.value][C.value] = opq((a16 ^ (64 * C.value)) + bb * 8192 + R.value * 81920); });
  });
  const u32 pown = opq(lds0 + lane * 16 + (PS1 + 2 * bb) * 8192 + hg * 2048);
  const u32 aK = lds0 + KS0 * 8192 + i16 * 128 + (((g >> 1) ^ (i16 >> 1)) * 16) + (g & 1) * 8;
  sfor<4>([&](auto I) { kr[I.value] = opq(aK ^ (32 * I.value)); });
  const u32 qwr = opq(lds0 + QX0 * 8192 + w * 2048 + lane * 16), qrd = opq(lds0 + (QX0 + w) * 8192 + lane * 16);
  __amdgpu_buffer_rsrc_t rp = __builtin_amdgcn_make_buffer_rsrc(pbase, 0, 8192, 0x00020000);
  const int po = (int)opq(w * 2048 + lane * 16);
  __amdgpu_buffer_rsrc_t ro = __builtin_amdgcn_make_buffer_rsrc(obase, 0, 512 * 1792, 0x00020000);
  const int oo = (int)opq(((2 * bp + bb) * 64 + 16 * hg + i16) * 1792 + (br * 448 + 64 * cg + 32 * ct + 16 * (g & 1) + 8 * (g >> 1)) * 2);

  auto pk44 = [](const f32x4& a, const f32x4& c) { return u32x4{pk2(a[0], a[1]), pk2(a[2], a[3]), pk2(c[0], c[1]), pk2(c[2], c[3])}; };
  auto weave = []() { sfor<12>([&](auto) { sgb<SG_DSR, 1>(); sgb<SG_MFMA, 1>(); }); sgb<SG_MFMA, 4>(); };
  s16x8 A[2][4], B[2][4];
  f32x4 sa[4] = {};
  {
    f32x4 qa[4][4] = {};
    u32 tq[2][4], xr[2][2];
    sfor<2>([&](auto H) {
      sfor<4>([&](auto C) { tq[H.value][C.value] = opq((aT ^ (32 * C.value)) + (w + 1) * 8192 + H.value * 81920); });
      sfor<2>([&](auto K) { xr[H.value][K.value] = opq((a16 ^ (64 * K.value)) + H.value * 81920); });
    });
    auto ld = [&](auto G) {
      constexpr int gs = G.value, s = gs >> 1, ks = gs & 1, hi = QG[s] >= 10, o = (QG[s] - 10 * hi) * 8192;
      sfor<4>([&](auto I) {
        A[ks][I.value] = ldstr(tq[hi][I.value] + o + ks * 4096);
        B[ks][I.value] = lds128(xr[hi][ks] + o + I.value * 2048);
      });
    };
    bar();
    __builtin_amdgcn_s_setprio(3);
    ld(IC(0));
    sfor<13>([&](auto G) {
      constexpr int gs = G.value, bf = gs & 1;
      sched0();
      if constexpr (bf == 1) { bar(); sched0(); }
      ld(IC(gs + 1));
      sfor<16>([&](auto I) { constexpr int d = I.value >> 2, q = I.value & 3; qa[d][q] = mfma16(A[bf][d], B[bf][q], qa[d][q]); });
      weave();
    });
    sched0(); bar_lds(); sched0();
    auto wrq = [&](auto I) {
      constexpr int m = I.value & 1, q = I.value >> 1;
      *(LAS u32x4*)(size_t)(qwr + q * 8192 + m * 1024) = pk44(qa[2 * m][q], qa[2 * m + 1][q]);
    };
    sfor<8>([&](auto I) {
      constexpr int m = I.value & 1, q = I.value >> 1;
      qa[2 * m][q] = mfma16(A[1][2 * m], B[1][q], qa[2 * m][q]);
      qa[2 * m + 1][q] = mfma16(A[1][2 * m + 1], B[1][q], qa[2 * m + 1][q]);
      sched0();
      if constexpr (I.value >= 2) wrq(IC(I.value - 2));
      sched0();
    });
    wrq(IC(6)); wrq(IC(7));
    sched0(); bar_lds(); sched0();
    s16x8 KF[8], QF[8];
    auto ldk = [&](auto F) {
      constexpr int f = F.value, u = f >> 2, o = (u >> 1) * 8192 + (f & 3) * 2048;
      if constexpr ((f & 3) == 0) QF[u] = lds128(qrd + u * 1024);
      s16x4 lo = *(const LAS s16x4*)(size_t)(kr[2 * (u & 1)] + o), hi = *(const LAS s16x4*)(size_t)(kr[2 * (u & 1) + 1] + o);
      KF[f & 7] = __builtin_shufflevector(lo, hi, 0, 1, 2, 3, 4, 5, 6, 7);
    };
    sfor<6>([&](auto F) { ldk(F); sched0(); });
    sfor<32>([&](auto F) {
      constexpr int f = F.value;
      sched0();
      if constexpr (f + 6 < 32) ldk(IC(f + 6));
      sa[f & 3] = mfma16(KF[f & 7], QF[f >> 2], sa[f & 3]);
    });
  }
  asm volatile("s_nop 7\n\ts_nop 3" : "+v"(sa[0]), "+v"(sa[1]), "+v"(sa[2]), "+v"(sa[3]));

  u32 ptg;
  {
    const float cs = 0.0625f * 1.44269504088896340736f;
    float m = vmax3(vmax3(sa[0][0], sa[0][1], sa[0][2]), vmax3(sa[0][3], sa[1][0], sa[1][1]), vmax3(sa[1][2], sa[1][3], sa[2][0]));
    m = vmax3(m, vmax3(sa[2][1], sa[2][2], sa[2][3]), vmax3(sa[3][0], sa[3][1], vmax(sa[3][2], sa[3][3])));
    m = xmax(m) * cs;
    ptg = (u32)((int)~epoch >> 31) & PTAG;
    asm volatile("" ::"v"(cpf));
    float e[16];
    sfor<16>([&](auto I) { constexpr int i = I.value; e[i] = __builtin_amdgcn_exp2f(fmaf(sa[i >> 2][i & 3], cs, -m)); });
    const float sum = (((e[0] + e[1]) + (e[2] + e[3])) + ((e[4] + e[5]) + (e[6] + e[7]))) + (((e[8] + e[9]) + (e[10] + e[11])) + ((e[12] + e[13]) + (e[14] + e[15])));
    const float r = __builtin_amdgcn_rcpf(xsum(sum));
    sfor<16>([&](auto I) { e[I.value] *= r; });
    sched0();
    u32x4 pf[2];
    sfor<8>([&](auto I) { pf[I.value >> 2][I.value & 3] = pk2(e[2 * I.value], e[2 * I.value + 1]); });
    sched0();
    sfor<8>([&](auto I) { pf[I.value >> 2][I.value & 3] = (pf[I.value >> 2][I.value & 3] & ~PTAG) | ptg; });
    __builtin_amdgcn_raw_buffer_store_b128(pf[0], rp, po, 0, 16);
    __builtin_amdgcn_raw_buffer_store_b128(pf[1], rp, po + 1024, 0, 16);
  }
  sched0(); bar(); sched0();
  if (!vact) return;

  f32x4 xa[4][4][2] = {};
  s16x8 PF[4], XF[16];
  auto ldv = [&](auto G) {
    constexpr int gv = G.value, u = gv >> 1, ks = gv & 1, hi = US[u][0] >= 10, vo = (US[u][0] - 10 * hi) * 8192;
    sfor<4>([&](auto I) {
      constexpr int sl = US[u][2 + (I.value >> 1)];
      A[ks][I.value] = lds128(va[hi][ks] + vo + I.value * 2048);
      B[ks][I.value] = ldstr(wa[I.value & 1][sl >> 3] + (sl & 7) * 8192 + ks * 4096);
    });
  };
  auto cvx = [&](auto I) {
    constexpr int i = I.value;
    u32x4 t = pk44(xa[i >> 2][i & 2][i & 1], xa[i >> 2][(i & 2) + 1][i & 1]);
    sfor<4>([&](auto E) { t[E.value] ^= ptg; });
    XF[i] = __builtin_bit_cast(s16x8, t);
  };
  const u32 pa[4] = {opq(pown ^ 2048), opq(pown ^ 4096), opq(pown ^ 6144), pown};
  auto ldp = [&](auto F) {
    constexpr int f = F.value, hd = fin_head(f);
    PF[f & 3] = lds128(pa[fin_part(f)] + ((hd & 1) + (hd < 2 ? PS0 - PS1 : 0)) * 8192 + (f & 1) * 1024);
  };
  ldv(IC(0));
  sfor<16>([&](auto G) {
    constexpr int gv = G.value, bf = gv & 1, j = (gv >> 1) & 1;
    sched0();
    if constexpr (bf == 1 && gv < 15) { bar(); sched0(); }
    if constexpr (gv < 15) ldv(IC(gv + 1));
    if constexpr (gv >= 14) sfor<4>([&](auto I) { cvx(IC(4 * (gv - 14) + I.value)); });
    sfor<16>([&](auto I) {
      constexpr int e2 = I.value >> 3, kt = (I.value >> 1) & 3, c2 = I.value & 1;
      xa[2 * j + e2][kt][c2] = mfma16(A[bf][kt], B[bf][2 * e2 + c2], xa[2 * j + e2][kt][c2]);
    });
    if constexpr (gv == 14) { sfor<12>([&](auto) { sgb<SG_DSR, 1>(); sgb<SG_MFMA, 1>(); sgb<SG_VALU, 2>(); }); sfor<4>([&](auto) { sgb<SG_MFMA, 1>(); sgb<SG_VALU, 2>(); }); }
    else if constexpr (gv == 15) { sfor<16>([&](auto) { sgb<SG_MFMA, 1>(); sgb<SG_VALU, 2>(); }); }
    else weave();
  });
  {
    sched0(); bar();
    sfor<4>([&](auto F) { ldp(F); });
    sched0();
    sfor<NCV0>([&](auto I) { cvx(IC(8 + I.value)); pin(XF[8 + I.value]); });
    f32x4 oa[4][2] = {};
    auto send = [&](auto D) {
      constexpr int d = D.value;
      u32x4 a = pk44(oa[d - 1][0], oa[d - 1][1]);
      sfor<4>([&](auto E) { a[E.value] = umin(a[E.value], 0xFFFFFFFEu); });
      __builtin_amdgcn_raw_buffer_store_b128(a, rx, (xo ^ (d * 16384)) + (d - 1) * 1024, 0, 16);
    };
    sfor<32>([&](auto F) {
      constexpr int f = F.value, part = fin_part(f), x = 4 * fin_head(f) + 2 * (f & 1);
      sched0();
      if constexpr (f == 8 || f == 17) { bar(); sched0(); }
      const s16x8 pf = PF[f & 3];
      if constexpr (f + 4 < 32) ldp(IC(f + 4));
      oa[part][0] = mfma16(XF[x], pf, oa[part][0]);
      if constexpr (f + NCV0 < 8) { cvx(IC(8 + NCV0 + f)); sgb<SG_DSR, 1>(); sgb<SG_MFMA, 1>(); sgb<SG_VALU, 4>(); sgb<SG_MFMA, 1>(); sgb<SG_VALU, 4>(); }
      oa[part][1] = mfma16(XF[x + 1], pf, oa[part][1]);
      if constexpr (f == 17 || f == 21 || f == 25) send(IC((f - 13) / 4));
    });
    asm volatile("" : "+v"(oa[3][0]), "+v"(oa[3][1]));
    const u64 tc = rtnow();
    __builtin_amdgcn_s_sleep(4);
    u32x4 v[3];
    auto fetch = [&]() { sfor<3>([&](auto D) { v[D.value] = __builtin_amdgcn_raw_buffer_load_b128(rx, xo + D.value * 1024, 0, 16); }); };
    fetch();
#pragma nounroll
    for (u32 spins = 0; spins < SPIN_CAP; ++spins) {
      u32 mx = 0;
      sfor<3>([&](auto D) { sfor<4>([&](auto E) { mx = umax(mx, v[D.value][E.value]); }); });
      if (__all(mx != ~0u)) break;
      asm volatile("" ::: "memory");
      fetch();
    }
    float o[8];
    sfor<8>([&](auto I) {
      constexpr int i = I.value;
      auto up = [&](u32 x) { return __uint_as_float((i & 1) ? (x & 0xFFFF0000u) : (x << 16)); };
      o[i] = ((oa[3][i >> 2][i & 3] + up(v[0][i >> 1])) + up(v[1][i >> 1])) + up(v[2][i >> 1]);
    });
    const auto s0 = __builtin_amdgcn_permlane16_swap(pk2(o[0], o[1]), pk2(o[4], o[5]), false, false);
    const auto s1 = __builtin_amdgcn_permlane16_swap(pk2(o[2], o[3]), pk2(o[6], o[7]), false, false);
    const u32 w0 = s0[0], w1 = s1[0], w2 = s0[1], w3 = s1[1];
    __builtin_amdgcn_raw_buffer_store_b128(u32x4{w0, w1, w2, w3}, ro, oo, 0, 0);
    if (hg == 0 && cg == 0 && threadIdx.x == 256)
      __hip_atomic_fetch_xor((GAS u32*)ebase, PTAG, __ATOMIC_RELAXED, __HIP_MEMORY_SCOPE_AGENT);
    if (threadIdx.x == 256) { u64* st = (u64*)(p.ws + WS_STAMP) + bid * 8; st[0] = rt0; st[1] = rtnow(); st[3] = tc; }
  }
}

#define DEC 0
__global__ void decode(char* ws) {
  const u64* st = (const u64*)(ws + WS_STAMP);
  int bq = threadIdx.x; int idx = bq >> 3; int cg = 2 * (idx >> 3) + (idx & 1); bool act = cg < 7;
  __shared__ u64 mn, mx; __shared__ u64 sm;
  if (bq == 0) { mn = ~0ull; mx = 0; sm = 0; }
  __syncthreads();
  if (act) {
    atomicMin(&mn, st[bq * 8]);
    if (DEC == 1) atomicMax(&mx, st[bq * 8 + 1]);
    if (DEC == 2) atomicMax(&mx, st[bq * 8]);
    if (DEC == 3) atomicMax(&mx, st[bq * 8 + 2]);
    if (DEC == 4) atomicMax(&mx, st[bq * 8 + 3]);
  } else if (DEC == 5) atomicMax(&mx, st[bq * 8 + 2]);
  __syncthreads();
  u64 v = (DEC == 0) ? 0 : (mx - mn) * 10;
  if (v > 100000) v = 100000;
  u64 t0 = __builtin_amdgcn_s_memrealtime();
  while (__builtin_amdgcn_s_memrealtime() - t0 < v) __builtin_amdgcn_s_sleep(1);
}
extern "C" void kernel_launch(void* const* d_in, const int* in_sizes, int n_in, void* d_out, int out_size, void* d_ws,
                              size_t ws_size, hipStream_t stream) {
  Params p;
  for (int br = 0; br < 2; br++) {
    p.in[br][0] = (const char*)d_in[br * 5 + 0];
    p.in[br][1] = (const char*)d_in[br * 5 + 3];
    p.in[br][2] = (const char*)d_in[br * 5 + 1];
    p.in[br][3] = (const char*)d_in[br * 5 + 2];
    p.in[br][4] = (const char*)d_in[br * 5 + 4];
  }
  p.out = (char*)d_out;
  p.ws = (char*)d_ws;
  (void)hipFuncSetAttribute((const void*)fused_gqa2, hipFuncAttributeMaxDynamicSharedMemorySize, LDS_BYTES);
  fused_gqa2<<<256, 512, LDS_BYTES, stream>>>(p);
  decode<<<1, 256, 0, stream>>>((char*)d_ws);
}
```

```cpp
#include <hip/hip_runtime.h>
#include <utility>

typedef unsigned int u32;
typedef unsigned long long u64;
typedef short s16x4 __attribute__((ext_vector_type(4)));
typedef short s16x8 __attribute__((ext_vector_type(8)));
typedef float f32x2 __attribute__((ext_vector_type(2)));
typedef float f32x4 __attribute__((ext_vector_type(4)));
typedef u32 u32x4 __attribute__((ext_vector_type(4)));
typedef __bf16 bf16x2 __attribute__((ext_vector_type(2)));
#define LAS __attribute__((address_space(3)))
#define GAS __attribute__((address_space(1)))
#define DEV __device__ __forceinline__
#define IC(n) std::integral_constant<int, n>{}

constexpr size_t WS_EPOCH = 0;
constexpr size_t WS_FLAG = 4096;
constexpr size_t WS_P = 65536;
constexpr size_t WS_X = WS_P + 256 * 8192;
constexpr int LDS_BYTES = 163840;
constexpr u32 SPIN_CAP = 1u << 20;
constexpr u32 CODE_LAST = 18432;
constexpr u32 PTAG = 0x80008000u;
constexpr int NPF = 2, KPF = 3;
constexpr int NCV0 = 4;

struct Params {
  const char* in[2][5];
  char* out;
  char* ws;
};

template <class F, int... Is> DEV void sfor_impl(F&& f, std::integer_sequence<int, Is...>) { (f(std::integral_constant<int, Is>{}), ...); }
template <int N, class F> DEV void sfor(F&& f) { sfor_impl(f, std::make_integer_sequence<int, N>{}); }

template <int N> DEV void wait_vm() { asm volatile("s_waitcnt vmcnt(%0)" ::"n"(N) : "memory"); }
DEV void bar() { asm volatile("s_barrier" ::: "memory"); }
DEV void bar_lds() { asm volatile("s_waitcnt lgkmcnt(0)\n\ts_barrier" ::: "memory"); }
DEV void sched0() { __builtin_amdgcn_sched_barrier(0); }
DEV void pin(s16x8& x) { asm volatile("" : "+v"(x)); }
DEV u32 opq(u32 x) { asm volatile("" : "+v"(x)); return x; }
template <int M, int N> DEV void sgb() { __builtin_amdgcn_sched_group_barrier(M, N, 0); }
constexpr int SG_VALU = 0x2, SG_MFMA = 0x8, SG_DSR = 0x100;

template <int SC1> DEV void dma_block(u32 v0, u32 v1, u32 lds, const char* base) {
  if constexpr (SC1 == 0)
    asm volatile("s_mov_b32 m0, %2\n\ts_nop 0\n\tglobal_load_lds_dwordx4 %0, %3\n\tglobal_load_lds_dwordx4 %1, %3 offset:1024"
                 ::"v"(v0), "v"(v1), "s"(lds), "s"(base) : "memory");
  else
    asm volatile("s_mov_b32 m0, %2\n\ts_nop 0\n\tglobal_load_lds_dwordx4 %0, %3 sc1\n\tglobal_load_lds_dwordx4 %1, %3 offset:1024 sc1"
                 ::"v"(v0), "v"(v1), "s"(lds), "s"(base) : "memory");
}
DEV void dma_word(u32 v, u32 lds, const char* base) {
  asm volatile("s_mov_b32 m0, %1\n\ts_nop 0\n\tglobal_load_lds_dword %0, %2" ::"v"(v), "s"(lds), "s"(base) : "memory");
}
DEV void ld_word_async(u32& d, u32 off, const char* base) { asm volatile("global_load_dword %0, %1, %2 sc1" : "=v"(d) : "v"(off), "s"(base) : "memory"); }
DEV void st_x4_sc1(u32x4 v, u32 off, char* base) { asm volatile("global_store_dwordx4 %0, %1, %2 sc1\n\ts_nop 1" ::"v"(off), "v"(v), "s"(base) : "memory"); }
DEV void st_word_sc1(u32 v, u32 off, char* base) { asm volatile("global_store_dword %0, %1, %2 sc1" ::"v"(off), "v"(v), "s"(base) : "memory"); }
template <int N> DEV bool wait_all_eq(u32 d, u32 want) {
  u64 m;
  asm volatile("s_waitcnt vmcnt(%3)\n\tv_cmp_ne_u32 %0, %1, %2" : "=s"(m) : "v"(d), "v"(want), "n"(N) : "memory");
  return m == 0;
}
template <int N> DEV u32 wait_tag(u32 d) { u32 r; asm volatile("s_waitcnt vmcnt(%2)\n\tv_xor_b32 %0, 0x80008000, %1" : "=v"(r) : "v"(d), "n"(N) : "memory"); return r; }
DEV void spin_all_eq(u32 off, const char* base, u32 want) {
  for (u32 spins = 0; spins < SPIN_CAP; ++spins) {
    u64 m; u32 d;
    asm volatile("global_load_dword %1, %2, %3 sc1\n\ts_waitcnt vmcnt(0)\n\tv_cmp_ne_u32 %0, %1, %4" : "=s"(m), "=&v"(d) : "v"(off), "s"(base), "v"(want) : "memory");
    if (m == 0) break;
  }
}

DEV s16x8 lds128(u32 a) { return *(const LAS s16x8*)(size_t)a; }
DEV s16x8 ldstr(u32 a) {
  s16x4 lo = __builtin_amdgcn_ds_read_tr16_b64_v4i16((LAS s16x4*)(size_t)a);
  s16x4 hi = __builtin_amdgcn_ds_read_tr16_b64_v4i16((LAS s16x4*)(size_t)(a + 512));
  return __builtin_shufflevector(lo, hi, 0, 1, 2, 3, 4, 5, 6, 7);
}
DEV u32 pk2(float a, float b) { f32x2 t = {a, b}; return __builtin_bit_cast(u32, __builtin_convertvector(t, bf16x2)); }
DEV f32x4 mfma16(s16x8 a, s16x8 b, f32x4 c) { return __builtin_amdgcn_mfma_f32_16x16x32_bf16(a, b, c, 0, 0, 0); }
DEV float vmax(float a, float b) { float r; asm("v_max_f32 %0, %1, %2" : "=v"(r) : "v"(a), "v"(b)); return r; }
DEV float vmax3(float a, float b, float c) { float r; asm("v_max3_f32 %0, %1, %2, %3" : "=v"(r) : "v"(a), "v"(b), "v"(c)); return r; }
DEV float xmax(float m) {
  u32 u = __float_as_uint(m);
  auto s = __builtin_amdgcn_permlane32_swap(u, u, false, false);
  u32 a = s[0], b = s[1];
  m = vmax(__uint_as_float(a), __uint_as_float(b));
  u = __float_as_uint(m);
  auto t = __builtin_amdgcn_permlane16_swap(u, u, false, false);
  a = t[0]; b = t[1];
  return vmax(__uint_as_float(a), __uint_as_float(b));
}
DEV float xsum(float m) {
  u32 u = __float_as_uint(m);
  auto s = __builtin_amdgcn_permlane32_swap(u, u, false, false);
  u32 a = s[0], b = s[1];
  m = __uint_as_float(a) + __uint_as_float(b);
  u = __float_as_uint(m);
  auto t = __builtin_amdgcn_permlane16_swap(u, u, false, false);
  a = t[0]; b = t[1];
  return __uint_as_float(a) + __uint_as_float(b);
}
DEV u32 umin(u32 a, u32 b) { return a < b ? a : b; }
DEV u32 umax(u32 a, u32 b) { return a > b ? a : b; }

constexpr int QG[7] = {0, 5, 10, 15, 0, 5, 10};
constexpr int KS0 = 15, QX0 = 10;
constexpr int US[8][4] = {{0, 1, 19, 2}, {3, 4, 5, 6}, {7, 8, 9, 14}, {15, 16, 17, 18}, {10, 11, 12, 13}, {0, 1, 19, 2}, {3, 4, 5, 6}, {7, 8, 9, 14}};
constexpr int UO0[4] = {2, 0, 1, 3};
constexpr int PS0 = 15, PS1 = 10;
constexpr int imin(int a, int b) { return a < b ? a : b; }
constexpr int imax(int a, int b) { return a > b ? a : b; }
constexpr int NEED[17] = {5, 10, 15, 20, 25, 30, 35, 35, 39, 43, 47, 51, 55, 59, 63, 67, 71};
constexpr int ALLOW[17] = {20, 20, 20, 25, 30, 35, 40, 45, 51, 51, 59, 59, 63, 67, 71, 75, 79};
constexpr int LOOK[17] = {5, 5, 5, 5, 5, 5, 5, 7, 7, 6, 6, 6, 6, 6, 6, 6, 6};
constexpr int ib(int k) { return k < 0 ? 0 : imax(ib(k - 1), imin(ALLOW[k], NEED[k] + LOOK[k])); }
constexpr int open_at(int k) { return 2 * (ib(k) - NEED[k]) + (k > 4 && NEED[k] <= ib(4) ? 1 : 0); }
static_assert(ib(4) < NEED[6] && ib(5) == 35 && ib(KPF - 1) == 5 * (KPF + 1), "");
constexpr int fin_part(int f) { return f < 12 ? f >> 2 : f < 24 ? (f - 12) >> 2 : 3; }
constexpr int fin_head(int f) { return f < 12 ? (f >> 1) & 1 : f < 24 ? 2 + ((f >> 1) & 1) : (f - 24) >> 1; }

__global__ __launch_bounds__(512, 2) void fused_gqa2(Params p) {
  extern __shared__ __attribute__((aligned(16))) char smem[];
  u64 pc0;
  asm volatile("s_getpc_b64 %0" : "=s"(pc0));
  u64 kx0, kq0;
  asm volatile("s_load_dwordx2 %0, %2, %3\n\ts_load_dwordx2 %1, %2, %3 offset:8" : "=&s"(kx0), "=&s"(kq0)
               : "s"((u64)__builtin_amdgcn_kernarg_segment_ptr()), "s"(((blockIdx.x >> 2) & 1) * 40));
  const u32 bid = blockIdx.x, xcd = bid & 7, idx = bid >> 3;
  const u32 br = xcd >> 2, hg = xcd & 3, b = idx & 7, hh = (idx >> 3) & 3, h = hg * 4 + hh;
  const u32 bp = b >> 1, cg = 2 * hh + (b & 1);
  const bool vact = cg < 7;
  const u32 lane = threadIdx.x & 63;
  const u32 wv = __builtin_amdgcn_readfirstlane(threadIdx.x >> 6);
  const u32 lds0 = (u32)(size_t)(LAS char*)smem;

  if (__builtin_expect(wv < 4, 1)) {
    const u32 L = wv;
    __builtin_amdgcn_s_setprio(2);
    const u32 r8 = lane >> 3, c = lane & 7, row = 16 * L + r8;
    const u32 cA0 = (c ^ (r8 >> 1)) * 16, cA1 = (c ^ (4 + (r8 >> 1))) * 16, cW0 = (c ^ (r8 & 2)) * 16, cW1 = (c ^ ((r8 & 2) | 4)) * 16;
    const u32 ox0 = row * 896 + cA0, ox1 = (row + 8) * 896 + cA1 - 1024;
    const u32 oq0 = row * 8192 + cW0, oq1 = (row + 8) * 8192 + cW1 - 1024;
    const u32 lb = lds0 + L * 2048;
    const u32 qrot = ((b + hh) & 3) * 7 / 4;
    const u32 xo0 = b * 64 * 896 + qrot * 128, qo0 = h * 512 + qrot * 64 * 8192;
    asm volatile("" ::"v"(ox0), "v"(ox1), "v"(oq0), "v"(oq1), "s"(lb), "s"(xo0), "s"(qo0));
    const char *pX, *pQ;
    asm volatile("s_waitcnt lgkmcnt(0)\n\ts_mov_b64 %0, %2\n\ts_mov_b64 %1, %3" : "=&s"(pX), "=&s"(pQ) : "s"(kx0), "s"(kq0));
    dma_block<0>(ox0, ox1, lb, pX + xo0);
    sfor<4>([&](auto T) { dma_block<0>(oq0, oq1, lb + (1 + T.value) * 8192, pQ + qo0 + T.value * 128); });
    sched0();
    const char *pK = p.in[br][2], *pV = p.in[br][3], *pO = p.in[br][4];
    char* ws = p.ws;
    const char* X = pX + (size_t)b * 64 * 896;
    const char* WQ = pQ + h * 512;
    const u32 ok0 = row * 512 + cA0, ok1 = (row + 8) * 512 + cA1 - 1024;
    const u32 oo0 = row * 896 + cW0, oo1 = (row + 8) * 896 + cW1 - 1024;
    const u32 op0 = L * 2048 + lane * 16;
    const char *KK, *VV, *WO, *PP;
    const u32 vrot = (bp + cg) & 3, krot = (hh & 1) * 2;

    u32 ep;
    auto issue = [&](auto I) {
      constexpr int i = I.value;
      if constexpr (i < 35) {
        constexpr int s = i / 5, t = i % 5;
        u32 ss = s + qrot; if (ss >= 7) ss -= 7;
        if constexpr (t == 0) dma_block<0>(ox0, ox1, lb + QG[s] * 8192, X + ss * 128);
        else dma_block<0>(oq0, oq1, lb + (QG[s] + t) * 8192, WQ + (size_t)ss * 64 * 8192 + (t - 1) * 128);
      } else if constexpr (i < 39) {
        const u32 kb = (i - 35) ^ krot;
        dma_block<0>(ok0, ok1, lb + (KS0 + kb) * 8192, KK + kb * 128);
      } else if constexpr (i < 71) {
        constexpr int u = (i - 39) / 4, t = u == 0 ? UO0[(i - 39) % 4] : (i - 39) % 4, j = u & 1;
        const u32 ss = ((u >> 1) + vrot) & 3;
        if constexpr (t < 2) dma_block<0>(ok0, ok1, lb + US[u][t] * 8192, VV + (size_t)(t * 8 + j) * 32768 + ss * 128);
        else dma_block<0>(oo0, oo1, lb + US[u][t] * 8192, WO + (size_t)((2 * j + t - 2) * 256 + ss * 64) * 896);
      } else {
        constexpr int t = i - 71;
        dma_block<1>(op0, op0, lb + ((t < 4 ? PS0 : PS1) + (t & 3)) * 8192, PP + (size_t)(((t >> 1) & 1) * 16 + (t >> 2) * 2 + (t & 1)) * 8192);
      }
    };
    auto issue_range = [&](auto A, auto B) { sfor<B.value - A.value>([&](auto J) { issue(IC(A.value + J.value)); }); };
    auto step = [&](auto K) {
      constexpr int k = K.value;
      issue_range(IC(ib(k - 1)), IC(ib(k)));
      wait_vm<open_at(k)>();
      bar();
    };

    issue_range(IC(5), IC(ib(0)));
    asm volatile("" : "+s"(pK), "+s"(pV), "+s"(pO), "+s"(ws));
    KK = pK + (size_t)(b * 8 + (h >> 1)) * 32768;
    VV = pV + (size_t)(bp * 16 + hg * 2) * 32768;
    WO = pO + (size_t)hg * 1024 * 896 + (vact ? cg : 0) * 128;
    PP = ws + WS_P + (size_t)((br * 8 + bp * 2) * 16 + hg * 4) * 8192;
    wait_vm<open_at(0)>();
    bar();
    sfor<4>([&](auto K) {
      constexpr int k = K.value + 1;
      if constexpr (k == KPF) {
        if (!vact) sfor<NPF>([&](auto J) { dma_word((((L >> 1) * 8 + (L & 1)) * 64 + J.value * 16) * 512 + lane * 128, lb + QG[k + 1] * 8192, VV); });
        issue_range(IC(ib(k - 1)), IC(ib(k)));
        if (!vact) wait_vm<open_at(k) + NPF>(); else wait_vm<open_at(k)>();
        bar();
      } else {
        step(IC(K.value + 1));
      }
    });
    ld_word_async(ep, 0u, ws + WS_EPOCH + (br * 4 + bp) * 256);
    step(IC(5));
    if (vact) {
      issue_range(IC(ib(5)), IC(ib(6)));
      const u32 tag = wait_tag<open_at(6)>(ep);
      bar();
      sfor<10>([&](auto K) { step(IC(K.value + 7)); });
      issue_range(IC(ib(16)), IC(79));
      __builtin_amdgcn_s_setprio(3);
      const u32 sgn = (u32)((int)tag >> 31);
      const bool neg = __builtin_amdgcn_readfirstlane(sgn) != 0;
      auto check = [&](auto H) {
        constexpr int hf = H.value;
        const u32 pv = opq(lds0 + op0 + (hf ? PS1 : PS0) * 8192);
        for (u32 spins = 0;; ++spins) {
          u32x4 v[8];
          sfor<4>([&](auto T) {
            if constexpr (hf) wait_vm<6 - 2 * T.value>(); else if constexpr (T.value == 0) wait_vm<8>();
            v[2 * T.value] = __builtin_bit_cast(u32x4, lds128(pv + T.value * 8192));
            v[2 * T.value + 1] = __builtin_bit_cast(u32x4, lds128(pv + T.value * 8192 + 1024));
          });
          u32 acc[2] = {sgn, sgn};
          if (neg) sfor<16>([&](auto I) { constexpr int i = I.value; acc[i & 1] = __builtin_amdgcn_bitop3_b32(acc[i & 1], v[i >> 1][2 * (i & 1)], v[i >> 1][2 * (i & 1) + 1], 0x80); });
          else sfor<16>([&](auto I) { constexpr int i = I.value; acc[i & 1] = __builtin_amdgcn_bitop3_b32(acc[i & 1], v[i >> 1][2 * (i & 1)], v[i >> 1][2 * (i & 1) + 1], 0xFE); });
          if (__all((((acc[0] ^ sgn) | (acc[1] ^ sgn)) & PTAG) == 0) || spins >= SPIN_CAP) break;
          issue_range(IC(71 + 4 * hf), IC(75 + 4 * hf));
          wait_vm<0>();
        }
      };
      check(IC(0));
      bar();
      check(IC(1));
      const char* FL = ws + WS_FLAG;
      const u32 fo = ((br * 4 + bp) * 4 + ((lane >> 2) & 3)) * 128 + (lane & 3) * 4;
      u32 fl;
      ld_word_async(fl, fo, FL);
      bar();
      if (!wait_all_eq<0>(fl, tag)) spin_all_eq(fo, FL, tag);
      bar();
    } else {
      issue_range(IC(ib(5)), IC(39));
      const u32 tag = wait_tag<8>(ep);
      bar();
      bar();
      wait_vm<0>();
      bar();
      char* ibx = ws + WS_X + (size_t)((br * 4 + bp) * 32 + hg) * 16384;
      const u32x4 e = {~0u, ~0u, ~0u, ~0u};
      const u32 mo = L * 4096 + lane * 16;
      sfor<3>([&](auto D) { st_x4_sc1(e, mo + D.value * 1024, ibx); });
      bar();
      for (u32 c2 = 1; c2 < 7; ++c2) sfor<3>([&](auto D) { st_x4_sc1(e, mo + D.value * 1024, ibx + (size_t)c2 * 65536); });
      wait_vm<0>();
      st_word_sc1(tag, L * 4, ws + WS_FLAG + ((br * 4 + bp) * 4 + hg) * 128);
    }
    return;
  }

  asm volatile("s_waitcnt lgkmcnt(0)" ::"s"(kx0), "s"(kq0));
  const u32 w = wv - 4;
  u32 cpf = 0;
  if (w == 0) cpf = *(const GAS u32*)((pc0 & ~127ull) + umin((idx % 5) * 4096 + lane * 64, CODE_LAST));
  char* xbase = p.ws + WS_X + (size_t)((br * 4 + bp) * 8 + cg) * 65536;
  char* pbase = p.ws + WS_P + (size_t)((br * 8 + b) * 16 + h) * 8192;
  char* obase = p.out;
  char* ebase = p.ws + WS_EPOCH + (br * 4 + bp) * 256;
  asm volatile("" : "+s"(xbase), "+s"(pbase), "+s"(obase), "+s"(ebase));
  const u32 epoch = __builtin_amdgcn_raw_buffer_load_b32(__builtin_amdgcn_make_buffer_rsrc(ebase, 0, 4, 0x00020000), (int)opq(0), 0, 16);
  __amdgpu_buffer_rsrc_t rx = __builtin_amdgcn_make_buffer_rsrc(xbase, 0, 65536, 0x00020000);
  const int xo = (int)opq(w * 4096 + lane * 16 + hg * 16384);

  const u32 i16 = lane & 15, g = lane >> 4, q4 = i16 >> 2, pp = lane & 3;
  const u32 a16 = lds0 + i16 * 128 + ((g ^ (i16 >> 1)) * 16);
  const u32 aT = lds0 + (8 * g + q4) * 128 + (((pp >> 1) ^ ((q4 & 2) | ((g & 1) << 2))) * 16) + 8 * (pp & 1);
  const u32 bb = w >> 1, ct = w & 1;
  u32 wa[2][3], va[2][2], kr[4];
  sfor<2>([&](auto C) {
    sfor<3>([&](auto R) { wa[C.value][R.value] = opq((aT ^ (64 * ct + 32 * C.value)) + R.value * 65536); });
    sfor<2>([&](auto R) { va[R.value][C.value] = opq((a16 ^ (64 * C.value)) + bb * 8192 + R.value * 81920); });
  });
  const u32 pown = opq(lds0 + lane * 16 + (PS1 + 2 * bb) * 8192 + hg * 2048);
  const u32 aK = lds0 + KS0 * 8192 + i16 * 128 + (((g >> 1) ^ (i16 >> 1)) * 16) + (g & 1) * 8;
  sfor<4>([&](auto I) { kr[I.value] = opq(aK ^ (32 * I.value)); });
  const u32 qwr = opq(lds0 + QX0 * 8192 + w * 2048 + lane * 16), qrd = opq(lds0 + (QX0 + w) * 8192 + lane * 16);
  __amdgpu_buffer_rsrc_t rp = __builtin_amdgcn_make_buffer_rsrc(pbase, 0, 8192, 0x00020000);
  const int po = (int)opq(w * 2048 + lane * 16);
  __amdgpu_buffer_rsrc_t ro = __builtin_amdgcn_make_buffer_rsrc(obase, 0, 512 * 1792, 0x00020000);
  const int oo = (int)opq(((2 * bp + bb) * 64 + 16 * hg + i16) * 1792 + (br * 448 + 64 * cg + 32 * ct + 16 * (g & 1) + 8 * (g >> 1)) * 2);

  auto pk44 = [](const f32x4& a, const f32x4& c) { return u32x4{pk2(a[0], a[1]), pk2(a[2], a[3]), pk2(c[0], c[1]), pk2(c[2], c[3])}; };
  auto weave = []() { sfor<12>([&](auto) { sgb<SG_DSR, 1>(); sgb<SG_MFMA, 1>(); }); sgb<SG_MFMA, 4>(); };
  s16x8 A[2][4], B[2][4];
  f32x4 sa[4] = {};
  {
    f32x4 qa[4][4] = {};
    u32 tq[2][4], xr[2][2];
    sfor<2>([&](auto H) {
      sfor<4>([&](auto C) { tq[H.value][C.value] = opq((aT ^ (32 * C.value)) + (w + 1) * 8192 + H.value * 81920); });
      sfor<2>([&](auto K) { xr[H.value][K.value] = opq((a16 ^ (64 * K.value)) + H.value * 81920); });
    });
    auto ld = [&](auto G) {
      constexpr int gs = G.value, s = gs >> 1, ks = gs & 1, hi = QG[s] >= 10, o = (QG[s] - 10 * hi) * 8192;
      sfor<4>([&](auto I) {
        A[ks][I.value] = ldstr(tq[hi][I.value] + o + ks * 4096);
        B[ks][I.value] = lds128(xr[hi][ks] + o + I.value * 2048);
      });
    };
    bar();
    __builtin_amdgcn_s_setprio(3);
    ld(IC(0));
    sfor<13>([&](auto G) {
      constexpr int gs = G.value, bf = gs & 1;
      sched0();
      if constexpr (bf == 1) { bar(); sched0(); }
      ld(IC(gs + 1));
      sfor<16>([&](auto I) { constexpr int d = I.value >> 2, q = I.value & 3; qa[d][q] = mfma16(A[bf][d], B[bf][q], qa[d][q]); });
      weave();
    });
    sched0(); bar_lds(); sched0();
    auto wrq = [&](auto I) {
      constexpr int m = I.value & 1, q = I.value >> 1;
      *(LAS u32x4*)(size_t)(qwr + q * 8192 + m * 1024) = pk44(qa[2 * m][q], qa[2 * m + 1][q]);
    };
    sfor<8>([&](auto I) {
      constexpr int m = I.value & 1, q = I.value >> 1;
      qa[2 * m][q] = mfma16(A[1][2 * m], B[1][q], qa[2 * m][q]);
      qa[2 * m + 1][q] = mfma16(A[1][2 * m + 1], B[1][q], qa[2 * m + 1][q]);
      sched0();
      if constexpr (I.value >= 2) wrq(IC(I.value - 2));
      sched0();
    });
    wrq(IC(6)); wrq(IC(7));
    sched0(); bar_lds(); sched0();
    s16x8 KF[8], QF[8];
    auto ldk = [&](auto F) {
      constexpr int f = F.value, u = f >> 2, o = (u >> 1) * 8192 + (f & 3) * 2048;
      if constexpr ((f & 3) == 0) QF[u] = lds128(qrd + u * 1024);
      s16x4 lo = *(const LAS s16x4*)(size_t)(kr[2 * (u & 1)] + o), hi = *(const LAS s16x4*)(size_t)(kr[2 * (u & 1) + 1] + o);
      KF[f & 7] = __builtin_shufflevector(lo, hi, 0, 1, 2, 3, 4, 5, 6, 7);
    };
    sfor<6>([&](auto F) { ldk(F); sched0(); });
    sfor<32>([&](auto F) {
      constexpr int f = F.value;
      sched0();
      if constexpr (f + 6 < 32) ldk(IC(f + 6));
      sa[f & 3] = mfma16(KF[f & 7], QF[f >> 2], sa[f & 3]);
    });
  }
  asm volatile("s_nop 7\n\ts_nop 3" : "+v"(sa[0]), "+v"(sa[1]), "+v"(sa[2]), "+v"(sa[3]));

  u32 ptg;
  {
    const float cs = 0.0625f * 1.44269504088896340736f;
    float m = vmax3(vmax3(sa[0][0], sa[0][1], sa[0][2]), vmax3(sa[0][3], sa[1][0], sa[1][1]), vmax3(sa[1][2], sa[1][3], sa[2][0]));
    m = vmax3(m, vmax3(sa[2][1], sa[2][2], sa[2][3]), vmax3(sa[3][0], sa[3][1], vmax(sa[3][2], sa[3][3])));
    m = xmax(m) * cs;
    ptg = (u32)((int)~epoch >> 31) & PTAG;
    asm volatile("" ::"v"(cpf));
    float e[16];
    sfor<16>([&](auto I) { constexpr int i = I.value; e[i] = __builtin_amdgcn_exp2f(fmaf(sa[i >> 2][i & 3], cs, -m)); });
    const float sum = (((e[0] + e[1]) + (e[2] + e[3])) + ((e[4] + e[5]) + (e[6] + e[7]))) + (((e[8] + e[9]) + (e[10] + e[11])) + ((e[12] + e[13]) + (e[14] + e[15])));
    const float r = __builtin_amdgcn_rcpf(xsum(sum));
    sfor<16>([&](auto I) { e[I.value] *= r; });
    sched0();
    u32x4 pf[2];
    sfor<8>([&](auto I) { pf[I.value >> 2][I.value & 3] = pk2(e[2 * I.value], e[2 * I.value + 1]); });
    sched0();
    sfor<8>([&](auto I) { pf[I.value >> 2][I.value & 3] = (pf[I.value >> 2][I.value & 3] & ~PTAG) | ptg; });
    __builtin_amdgcn_raw_buffer_store_b128(pf[0], rp, po, 0, 16);
    __builtin_amdgcn_raw_buffer_store_b128(pf[1], rp, po + 1024, 0, 16);
  }
  sched0(); bar(); sched0();
  if (!vact) return;

  f32x4 xa[4][4][2] = {};
  s16x8 PF[4], XF[16];
  auto ldv = [&](auto G) {
    constexpr int gv = G.value, u = gv >> 1, ks = gv & 1, hi = US[u][0] >= 10, vo = (US[u][0] - 10 * hi) * 8192;
    sfor<4>([&](auto I) {
      constexpr int sl = US[u][2 + (I.value >> 1)];
      A[ks][I.value] = lds128(va[hi][ks] + vo + I.value * 2048);
      B[ks][I.value] = ldstr(wa[I.value & 1][sl >> 3] + (sl & 7) * 8192 + ks * 4096);
    });
  };
  auto cvx = [&](auto I) {
    constexpr int i = I.value;
    u32x4 t = pk44(xa[i >> 2][i & 2][i & 1], xa[i >> 2][(i & 2) + 1][i & 1]);
    sfor<4>([&](auto E) { t[E.value] ^= ptg; });
    XF[i] = __builtin_bit_cast(s16x8, t);
  };
  const u32 pa[4] = {opq(pown ^ 2048), opq(pown ^ 4096), opq(pown ^ 6144), pown};
  auto ldp = [&](auto F) {
    constexpr int f = F.value, hd = fin_head(f);
    PF[f & 3] = lds128(pa[fin_part(f)] + ((hd & 1) + (hd < 2 ? PS0 - PS1 : 0)) * 8192 + (f & 1) * 1024);
  };
  ldv(IC(0));
  sfor<16>([&](auto G) {
    constexpr int gv = G.value, bf = gv & 1, j = (gv >> 1) & 1;
    sched0();
    if constexpr (bf == 1 && gv < 15) { bar(); sched0(); }
    if constexpr (gv < 15) ldv(IC(gv + 1));
    if constexpr (gv >= 14) sfor<4>([&](auto I) { cvx(IC(4 * (gv - 14) + I.value)); });
    sfor<16>([&](auto I) {
      constexpr int e2 = I.value >> 3, kt = (I.value >> 1) & 3, c2 = I.value & 1;
      xa[2 * j + e2][kt][c2] = mfma16(A[bf][kt], B[bf][2 * e2 + c2], xa[2 * j + e2][kt][c2]);
    });
    if constexpr (gv == 14) { sfor<12>([&](auto) { sgb<SG_DSR, 1>(); sgb<SG_MFMA, 1>(); sgb<SG_VALU, 2>(); }); sfor<4>([&](auto) { sgb<SG_MFMA, 1>(); sgb<SG_VALU, 2>(); }); }
    else if constexpr (gv == 15) { sfor<16>([&](auto) { sgb<SG_MFMA, 1>(); sgb<SG_VALU, 2>(); }); }
    else weave();
  });
  {
    sched0(); bar();
    sfor<4>([&](auto F) { ldp(F); });
    sched0();
    sfor<NCV0>([&](auto I) { cvx(IC(8 + I.value)); pin(XF[8 + I.value]); });
    f32x4 oa[4][2] = {};
    auto send = [&](auto D) {
      constexpr int d = D.value;
      u32x4 a = pk44(oa[d - 1][0], oa[d - 1][1]);
      sfor<4>([&](auto E) { a[E.value] = umin(a[E.value], 0xFFFFFFFEu); });
      __builtin_amdgcn_raw_buffer_store_b128(a, rx, (xo ^ (d * 16384)) + (d - 1) * 1024, 0, 16);
    };
    sfor<32>([&](auto F) {
      constexpr int f = F.value, part = fin_part(f), x = 4 * fin_head(f) + 2 * (f & 1);
      sched0();
      if constexpr (f == 8 || f == 17) { bar(); sched0(); }
      const s16x8 pf = PF[f & 3];
      if constexpr (f + 4 < 32) ldp(IC(f + 4));
      oa[part][0] = mfma16(XF[x], pf, oa[part][0]);
      if constexpr (f + NCV0 < 8) { cvx(IC(8 + NCV0 + f)); sgb<SG_DSR, 1>(); sgb<SG_MFMA, 1>(); sgb<SG_VALU, 4>(); sgb<SG_MFMA, 1>(); sgb<SG_VALU, 4>(); }
      oa[part][1] = mfma16(XF[x + 1], pf, oa[part][1]);
      if constexpr (f == 17 || f == 21 || f == 25) send(IC((f - 13) / 4));
    });
    asm volatile("" : "+v"(oa[3][0]), "+v"(oa[3][1]));
    __builtin_amdgcn_s_sleep(4);
    u32x4 v[3];
    auto fetch = [&]() { sfor<3>([&](auto D) { v[D.value] = __builtin_amdgcn_raw_buffer_load_b128(rx, xo + D.value * 1024, 0, 16); }); };
    fetch();
#pragma nounroll
    for (u32 spins = 0; spins < SPIN_CAP; ++spins) {
      u32 mx = 0;
      sfor<3>([&](auto D) { sfor<4>([&](auto E) { mx = umax(mx, v[D.value][E.value]); }); });
      if (__all(mx != ~0u)) break;
      asm volatile("" ::: "memory");
      fetch();
    }
    float o[8];
    sfor<8>([&](auto I) {
      constexpr int i = I.value;
      auto up = [&](u32 x) { return __uint_as_float((i & 1) ? (x & 0xFFFF0000u) : (x << 16)); };
      o[i] = ((oa[3][i >> 2][i & 3] + up(v[0][i >> 1])) + up(v[1][i >> 1])) + up(v[2][i >> 1]);
    });
    const auto s0 = __builtin_amdgcn_permlane16_swap(pk2(o[0], o[1]), pk2(o[4], o[5]), false, false);
    const auto s1 = __builtin_amdgcn_permlane16_swap(pk2(o[2], o[3]), pk2(o[6], o[7]), false, false);
    const u32 w0 = s0[0], w1 = s1[0], w2 = s0[1], w3 = s1[1];
    __builtin_amdgcn_raw_buffer_store_b128(u32x4{w0, w1, w2, w3}, ro, oo, 0, 0);
    if (hg == 0 && cg == 0 && threadIdx.x == 256)
      __hip_atomic_fetch_xor((GAS u32*)ebase, PTAG, __ATOMIC_RELAXED, __HIP_MEMORY_SCOPE_AGENT);
  }
}

extern "C" void kernel_launch(void* const* d_in, const int* in_sizes, int n_in, void* d_out, int out_size, void* d_ws,
                              size_t ws_size, hipStream_t stream) {
  Params p;
  for (int br = 0; br < 2; br++) {
    p.in[br][0] = (const char*)d_in[br * 5 + 0];
    p.in[br][1] = (const char*)d_in[br * 5 + 3];
    p.in[br][2] = (const char*)d_in[br * 5 + 1];
    p.in[br][3] = (const char*)d_in[br * 5 + 2];
    p.in[br][4] = (const char*)d_in[br * 5 + 4];
  }
  p.out = (char*)d_out;
  p.ws = (char*)d_ws;
  (void)hipFuncSetAttribute((const void*)fused_gqa2, hipFuncAttributeMaxDynamicSharedMemorySize, LDS_BYTES);
  fused_gqa2<<<256, 512, LDS_BYTES, stream>>>(p);
}
```
